```python
import torch

def _softmax(x, axis=-1):
    return torch.softmax(x, dim=axis)
def _gelu(x):
    return torch.nn.functional.gelu(x, approximate="tanh")
def _silu(x):
    return torch.nn.functional.silu(x)
def _sigmoid(x):
    return torch.sigmoid(x)
def _layernorm(x, axis=-1, eps=1e-5):
    m = x.mean(dim=axis, keepdim=True)
    v = x.var(dim=axis, keepdim=True, unbiased=False)
    return (x - m) / torch.sqrt(v + eps)
def _scatter_add(tgt, idx, src, axis=0):
    return tgt.clone().index_add_(axis, idx, src)
def _segment_sum(src, seg, n):
    out = torch.zeros((n,) + src.shape[1:], dtype=src.dtype)
    return out.index_add_(0, seg, src)
def _pad(x, pw):
    flat = tuple(v for pair in reversed(pw) for v in pair)
    return torch.nn.functional.pad(x, flat)

kernel_name = 'standalone__weight_only_int_omniquant__prompt'

def setup_inputs():

    _w = (lambda a: (lambda t: t.to(torch.bfloat16) if t.is_floating_point() else t)(torch.as_tensor(a)))
    B, S, D, F = 1, 2048, 1024, 3584
    bits = 8
    G = 32 if 32 > 0 else D
    D = (D // G) * G
    ng = D // G
    qmax = (1 << bits) - 1
    qw = torch.randint(0, qmax + 1, (D, F)).to(torch.int32)
    scales = (torch.abs((lambda *s: torch.randn(*s, dtype=torch.bfloat16) * 0.05)(ng, F)) * 0.05 + 0.01).to(float)
    zeros = torch.randint(0, qmax + 1, (ng, F)).to(float)
    act_scale = (torch.abs((lambda *s: torch.randn(*s, dtype=torch.bfloat16) * 0.05)(D)) * 0.5 + 0.5).to(float)
    return {
        "x": _w((lambda *s: torch.randn(*s, dtype=torch.bfloat16) * 0.05)(B, S, D)),
        "qw": _w(qw),
        "scales": _w(scales),
        "zeros": _w(zeros),
        "act_scale": _w(act_scale),
    }

def reference(x, qw, scales, zeros, act_scale):
    B, S, D = x.shape
    ng, F = scales.shape
    G = D // ng
    s = (lambda x, n, axis: torch.repeat_interleave(x, n, dim=axis))(scales, G, axis=0)
    z = (lambda x, n, axis: torch.repeat_interleave(x, n, dim=axis))(zeros, G, axis=0)
    w = ((lambda x, dt: x.to(dt))(qw, x.dtype) - z) * s
    xs = x * (lambda x, dt: x.to(dt))(act_scale, x.dtype)
    return torch.einsum("bsd,df->bsf", xs, w)
```

```cpp
#include <hip/hip_runtime.h>
#include <hip/hip_bf16.h>
typedef unsigned short bf16_t;
typedef short bf16x8 __attribute__((ext_vector_type(8)));
typedef float f32x4 __attribute__((ext_vector_type(4)));
constexpr int M = 2048, K = 1024, N = 3584, G = 32;
__device__ __forceinline__ float bf2f(bf16_t v) { return __uint_as_float(((unsigned)v) << 16); }
__device__ __forceinline__ bf16_t f2bf(float f) { __hip_bfloat16 h = __float2bfloat16(f); return *reinterpret_cast<bf16_t*>(&h); }
__global__ void prep_w(const int* qw, const bf16_t* sc, const bf16_t* ze, bf16_t* wt) {
    int n = blockIdx.x * blockDim.x + threadIdx.x; int k = blockIdx.y;
    float q = bf2f(f2bf((float)qw[k * N + n]));
    float d = bf2f(f2bf(q - bf2f(ze[(k / G) * N + n])));
    wt[n * K + k] = f2bf(d * bf2f(sc[(k / G) * N + n]));
}
__global__ void prep_x(const bf16_t* x, const bf16_t* a, bf16_t* xs) {
    int i = blockIdx.x * blockDim.x + threadIdx.x;
    xs[i] = f2bf(bf2f(x[i]) * bf2f(a[i % K]));
}
__global__ void gemm(const bf16_t* A, const bf16_t* Bt, bf16_t* C) {
    int lane = threadIdx.x & 63; int w = threadIdx.x >> 6;
    int m0 = blockIdx.y * 16, n0 = (blockIdx.x * 4 + w) * 16;
    f32x4 acc = {0,0,0,0};
    for (int k = 0; k < K; k += 32) {
        bf16x8 a = *(const bf16x8*)(A + (m0 + (lane & 15)) * K + k + 8 * (lane >> 4));
        bf16x8 b = *(const bf16x8*)(Bt + (n0 + (lane & 15)) * K + k + 8 * (lane >> 4));
        acc = __builtin_amdgcn_mfma_f32_16x16x32_bf16(a, b, acc, 0, 0, 0);
    }
    for (int j = 0; j < 4; ++j) C[(m0 + (lane >> 4) * 4 + j) * N + n0 + (lane & 15)] = f2bf(acc[j]);
}
extern "C" void kernel_launch(void* const* d_in, const int* in_sizes, int n_in, void* d_out, int out_size, void* d_ws, size_t ws_size, hipStream_t stream) {
    bf16_t* wt = (bf16_t*)d_ws; bf16_t* xs = wt + N * K;
    prep_w<<<dim3(N / 256, K), 256, 0, stream>>>((const int*)d_in[1], (const bf16_t*)d_in[2], (const bf16_t*)d_in[3], wt);
    prep_x<<<M * K / 256, 256, 0, stream>>>((const bf16_t*)d_in[0], (const bf16_t*)d_in[4], xs);
    gemm<<<dim3(N / 64, M / 16), 256, 0, stream>>>(xs, wt, (bf16_t*)d_out);
}
```

```cpp
#include <hip/hip_runtime.h>

__global__ __launch_bounds__(512, 2) void wq_gemm_kernel(const void* x, const void* qw, const void* sc, const void* ze, const void* act, void* ws, void* out) {
    __shared__ __attribute__((aligned(16))) unsigned char lds[157696];
    auto ka = __builtin_amdgcn_kernarg_segment_ptr();
    const unsigned l0 = (unsigned)(size_t)lds;
    asm volatile(
        "s_getpc_b64 s[18:19]\n\t"
        "L_pc_%=:\n\t"
        "s_load_dwordx2 s[60:61], %[ka], 8\n\t"
        "s_load_dwordx2 s[64:65], %[ka], 16\n\t"
        "s_load_dwordx2 s[68:69], %[ka], 24\n\t"
        "s_load_dwordx2 s[72:73], %[ka], 32\n\t"
        "s_load_dwordx2 s[20:21], %[ka], 0\n\t"
        "s_load_dwordx2 s[14:15], %[ka], 40\n\t"
        "s_load_dwordx2 s[28:29], %[ka], 48\n\t"
        "v_lshrrev_b32 v212, 6, %[tid]\n\t"
        "v_and_b32 v213, 63, %[tid]\n\t"
        "s_and_b32 s53, %[bid], 7\n\t"
        "s_lshr_b32 s54, %[bid], 3\n\t"
        "v_readfirstlane_b32 s51, v212\n\t"
        "s_and_b32 s35, s53, 1\n\t"
        "s_lshl_b32 s35, s35, 2\n\t"
        "s_and_b32 s36, s54, 3\n\t"
        "s_add_u32 s36, s36, s35\n\t"
        "s_lshr_b32 s35, s53, 1\n\t"
        "s_lshl_b32 s35, s35, 3\n\t"
        "s_lshr_b32 s37, s54, 2\n\t"
        "s_add_u32 s37, s37, s35\n\t"
        "s_and_b32 s52, s51, 3\n\t"
        "s_mov_b32 s62, 14680064\n\t"
        "s_mov_b32 s63, 0x20000\n\t"
        "s_mov_b32 s66, 229376\n\t"
        "s_mov_b32 s67, 0x20000\n\t"
        "s_mov_b32 s70, 229376\n\t"
        "s_mov_b32 s71, 0x20000\n\t"
        "s_mov_b32 s74, 2048\n\t"
        "s_mov_b32 s75, 0x20000\n\t"
        "s_mov_b32 s22, 4194304\n\t"
        "s_mov_b32 s23, 0x20000\n\t"
        "s_mov_b32 s30, 14680064\n\t"
        "s_mov_b32 s31, 0x20000\n\t"
        "s_mov_b32 s26, 229376\n\t"
        "s_mov_b32 s27, 0x20000\n\t"
        "s_mov_b32 s78, 4096\n\t"
        "s_mov_b32 s79, 0x20000\n\t"
        "v_cmp_lt_u32 vcc, 27, v213\n\t"
        "v_subrev_u32 v215, 28, v213\n\t"
        "s_lshl_b32 s35, s52, 1\n\t"
        "v_cndmask_b32 v215, v213, v215, vcc\n\t"
        "v_mov_b32 v214, s35\n\t"
        "v_addc_co_u32 v214, vcc, 0, v214, vcc\n\t"
        "v_mul_u32_u24 v192, 114688, v214\n\t"
        "v_lshl_add_u32 v192, v215, 4, v192\n\t"
        "s_mul_i32 s35, s37, 448\n\t"
        "v_add_u32 v192, s35, v192\n\t"
        "v_lshrrev_b32 v193, 2, v214\n\t"
        "v_mul_u32_u24 v193, 7168, v193\n\t"
        "v_lshl_add_u32 v193, v215, 3, v193\n\t"
        "s_mul_i32 s35, s37, 224\n\t"
        "v_add_u32 v193, s35, v193\n\t"
        "v_lshlrev_b32 v194, 4, v214\n\t"
        "v_cmp_gt_u32 s[40:41], 56, v213\n\t"
        "v_mov_b32 v216, 0x7ff00000\n\t"
        "s_nop 0\n\t"
        "v_cndmask_b32 v192, v216, v192, s[40:41]\n\t"
        "v_cndmask_b32 v193, v216, v193, s[40:41]\n\t"
        "v_cndmask_b32 v194, v216, v194, s[40:41]\n\t"
        "v_lshrrev_b32 v216, 3, v213\n\t"
        "v_lshrrev_b32 v217, 4, v213\n\t"
        "v_and_b32 v206, 7, v213\n\t"
        "v_xor_b32 v206, v206, v217\n\t"
        "v_lshlrev_b32 v206, 4, v206\n\t"
        "v_lshl_add_u32 v206, v216, 11, v206\n\t"
        "s_lshl_b32 s35, s36, 8\n\t"
        "s_lshl_b32 s38, s52, 6\n\t"
        "s_add_u32 s35, s35, s38\n\t"
        "s_lshl_b32 s35, s35, 11\n\t"
        "v_add_u32 v206, s35, v206\n\t"
        "v_xor_b32 v207, 64, v206\n\t"
        "v_add_u32 v207, 15360, v207\n\t"
        "v_add_u32 v208, 30720, v206\n\t"
        "v_add_u32 v209, 30720, v207\n\t"
        "s_lshl_b32 s39, s52, 13\n\t"
        "v_mov_b32 v210, 0\n\t"
        "s_cmp_ge_u32 s51, 4\n\t"
        "s_cbranch_scc1 L_prod_%=\n\t"
        "s_lshl_b32 s45, s36, 7\n\t"
        "s_waitcnt lgkmcnt(0)\n\t"
        "s_mul_i32 s81, s36, 917504\n\t"
        "s_mul_i32 s82, s36, 14336\n\t"
        "s_lshl_b32 s83, s36, 7\n\t"
        "buffer_load_dwordx2 v[144:145], v193, s[64:67], s82 offen\n\t"
        "buffer_load_dwordx2 v[146:147], v193, s[68:71], s82 offen\n\t"
        "buffer_load_dwordx4 v[148:151], v194, s[72:75], s83 offen\n\t"
        "buffer_load_dwordx4 v[112:115], v192, s[60:63], s81 offen nt\n\t"
        "s_add_u32 s81, s81, 14336\n\t"
        "buffer_load_dwordx4 v[116:119], v192, s[60:63], s81 offen nt\n\t"
        "s_add_u32 s81, s81, 14336\n\t"
        "buffer_load_dwordx4 v[120:123], v192, s[60:63], s81 offen nt\n\t"
        "s_add_u32 s81, s81, 14336\n\t"
        "buffer_load_dwordx4 v[124:127], v192, s[60:63], s81 offen nt\n\t"
        "s_add_u32 s81, s81, 14336\n\t"
        "buffer_load_dwordx4 v[128:131], v192, s[60:63], s81 offen nt\n\t"
        "s_add_u32 s81, s81, 14336\n\t"
        "buffer_load_dwordx4 v[132:135], v192, s[60:63], s81 offen nt\n\t"
        "s_add_u32 s81, s81, 14336\n\t"
        "buffer_load_dwordx4 v[136:139], v192, s[60:63], s81 offen nt\n\t"
        "s_add_u32 s81, s81, 14336\n\t"
        "buffer_load_dwordx4 v[140:143], v192, s[60:63], s81 offen nt\n\t"
        "s_add_u32 m0, s39, 0\n\t"
        "s_nop 0\n\t"
        "buffer_load_dwordx4 v206, s[20:23], s45 offen offset:0  lds\n\t"
        "buffer_load_dwordx4 v207, s[20:23], s45 offen offset:1024  lds\n\t"
        "buffer_load_dwordx4 v208, s[20:23], s45 offen offset:2048  lds\n\t"
        "buffer_load_dwordx4 v209, s[20:23], s45 offen offset:3072  lds\n\t"
        "s_add_u32 m0, s39, 4096\n\t"
        "s_add_u32 s46, s45, 65536\n\t"
        "buffer_load_dwordx4 v206, s[20:23], s46 offen offset:0  lds\n\t"
        "buffer_load_dwordx4 v207, s[20:23], s46 offen offset:1024  lds\n\t"
        "buffer_load_dwordx4 v208, s[20:23], s46 offen offset:2048  lds\n\t"
        "buffer_load_dwordx4 v209, s[20:23], s46 offen offset:3072  lds\n\t"
        "s_barrier\n\t"
        "v_lshrrev_b32 v216, 3, v215\n\t"
        "v_lshlrev_b32 v216, 5, v216\n\t"
        "v_and_b32 v217, 1, v215\n\t"
        "v_lshl_add_u32 v216, v217, 4, v216\n\t"
        "v_bfe_u32 v217, v215, 1, 2\n\t"
        "v_lshl_add_u32 v216, v217, 2, v216\n\t"
        "v_lshlrev_b32 v217, 2, v215\n\t"
        "v_cmp_gt_u32 vcc, 24, v215\n\t"
        "s_nop 1\n\t"
        "v_cndmask_b32 v216, v217, v216, vcc\n\t"
        "v_bfe_u32 v217, v216, 1, 3\n\t"
        "v_xor_b32 v217, v217, v214\n\t"
        "v_lshlrev_b32 v217, 4, v217\n\t"
        "v_lshl_add_u32 v195, v216, 7, v217\n\t"
        "v_xor_b32 v196, 16, v195\n\t"
        "v_add_u32 v196, 256, v196\n\t"
        "v_lshlrev_b32 v216, 4, v213\n\t"
        "v_add_u32 v216, 140416, v216\n\t"
        "v_add_u32 v242, 32768, v195\n\t"
        "v_add_u32 v243, 32768, v196\n\t"
        "v_cndmask_b32 v242, v216, v242, s[40:41]\n\t"
        "v_cndmask_b32 v243, v216, v243, s[40:41]\n\t"
        "s_mul_i32 s35, s37, 4096\n\t"
        "s_add_u32 s35, s35, 8388608\n\t"
        "s_add_u32 s76, s14, s35\n\t"
        "s_addc_u32 s77, s15, 0\n\t"
        "buffer_load_dwordx2 v[198:199], v210, s[76:79], 0 offen offset:2048 sc1\n\t"
        "s_mul_i32 s35, s37, 229376\n\t"
        "s_add_u32 s24, s14, s35\n\t"
        "s_addc_u32 s25, s15, 0\n\t"
        "s_lshl_b32 s90, s36, 7\n\t"
        "s_lshl_b32 s35, s52, 3\n\t"
        "s_add_u32 s90, s90, s35\n\t"
        "v_and_b32 v214, 15, v213\n\t"
        "v_lshrrev_b32 v215, 4, v213\n\t"
        "v_lshrrev_b32 v216, 1, v214\n\t"
        "v_xor_b32 v216, v216, v215\n\t"
        "v_lshlrev_b32 v216, 4, v216\n\t"
        "v_lshl_add_u32 v234, v214, 7, v216\n\t"
        "v_xor_b32 v235, 64, v234\n\t"
        "v_add_u32 v232, s39, v234\n\t"
        "v_add_u32 v233, s39, v235\n\t"
        "s_lshl_b32 s35, s52, 5\n\t"
        "v_add_u32 v217, s35, v214\n\t"
        "v_mul_u32_u24 v217, 240, v217\n\t"
        "v_add_u32 v217, 47104, v217\n\t"
        "v_lshl_add_u32 v236, v215, 4, v217\n\t"
        "v_lshl_add_u32 v237, v215, 3, v217\n\t"
        "s_mul_i32 s35, s52, 3584\n\t"
        "v_lshlrev_b32 v240, 4, v213\n\t"
        "v_add_u32 v240, s35, v240\n\t"
        "v_add_u32 v241, 32768, v240\n\t"
        "s_mul_i32 s35, s52, 7680\n\t"
        "s_add_u32 s44, s35, 77824\n\t"
        "s_add_u32 s43, s35, 126976\n\t"
        "s_cmp_ge_u32 s52, 2\n\t"
        "s_cselect_b32 s44, s43, s44\n\t"
        "s_sub_u32 s43, s44, s35\n\t"
        "s_sub_u32 s43, s43, 47104\n\t"
        "v_add_u32 v238, s43, v236\n\t"
        "v_add_u32 v239, s43, v237\n\t"
        "s_lshl_b32 s49, s52, 6\n\t"
        "s_add_u32 s49, s49, 32\n\t"
        "s_movk_i32 s50, 240\n\t"
        "v_mul_u32_u24 v214, 0x1556, v213\n\t"
        "v_lshrrev_b32 v214, 16, v214\n\t"
        "v_mul_u32_u24 v215, 12, v214\n\t"
        "v_sub_u32 v215, v213, v215\n\t"
        "s_and_b32 s39, s37, 1\n\t"
        "s_lshl_b32 s41, s39, 5\n\t"
        "v_lshlrev_b32 v215, 4, v215\n\t"
        "v_add_u32 v215, s41, v215\n\t"
        "v_mad_u32_u24 v206, v214, s50, v215\n\t"
        "v_add_u32 v206, s44, v206\n\t"
        "s_lshl_b32 s35, s36, 8\n\t"
        "s_add_u32 s42, s35, s49\n\t"
        "v_add_u32 v214, s42, v214\n\t"
        "v_mul_u32_u24 v214, 7168, v214\n\t"
        "s_mul_i32 s35, s37, 224\n\t"
        "v_add3_u32 v207, v214, v215, s35\n\t"
        "s_cmp_eq_u32 s39, 0\n\t"
        "s_cselect_b32 s41, 192, 0\n\t"
        "v_lshrrev_b32 v214, 1, v213\n\t"
        "v_and_b32 v215, 1, v213\n\t"
        "v_lshlrev_b32 v215, 4, v215\n\t"
        "v_add_u32 v215, s41, v215\n\t"
        "v_mad_u32_u24 v208, v214, s50, v215\n\t"
        "v_add_u32 v208, s44, v208\n\t"
        "v_add_u32 v214, s42, v214\n\t"
        "v_mul_u32_u24 v214, 7168, v214\n\t"
        "v_add3_u32 v209, v214, v215, s35\n\t"
        "v_mov_b32 v244, v206\n\t"
        "v_mov_b32 v245, v207\n\t"
        "v_mov_b32 v246, v208\n\t"
        "v_mov_b32 v247, v209\n\t"
        "v_mov_b32 v250, 0\n\t"
        "v_mov_b32 v0, 0\n\t"
        "v_mov_b32 v1, 0\n\t"
        "v_mov_b32 v2, 0\n\t"
        "v_mov_b32 v3, 0\n\t"
        "v_mov_b32 v4, 0\n\t"
        "v_mov_b32 v5, 0\n\t"
        "v_mov_b32 v6, 0\n\t"
        "v_mov_b32 v7, 0\n\t"
        "v_mov_b32 v8, 0\n\t"
        "v_mov_b32 v9, 0\n\t"
        "v_mov_b32 v10, 0\n\t"
        "v_mov_b32 v11, 0\n\t"
        "v_mov_b32 v12, 0\n\t"
        "v_mov_b32 v13, 0\n\t"
        "v_mov_b32 v14, 0\n\t"
        "v_mov_b32 v15, 0\n\t"
        "v_mov_b32 v16, 0\n\t"
        "v_mov_b32 v17, 0\n\t"
        "v_mov_b32 v18, 0\n\t"
        "v_mov_b32 v19, 0\n\t"
        "v_mov_b32 v20, 0\n\t"
        "v_mov_b32 v21, 0\n\t"
        "v_mov_b32 v22, 0\n\t"
        "v_mov_b32 v23, 0\n\t"
        "v_mov_b32 v24, 0\n\t"
        "v_mov_b32 v25, 0\n\t"
        "v_mov_b32 v26, 0\n\t"
        "v_mov_b32 v27, 0\n\t"
        "v_mov_b32 v28, 0\n\t"
        "v_mov_b32 v29, 0\n\t"
        "v_mov_b32 v30, 0\n\t"
        "v_mov_b32 v31, 0\n\t"
        "v_mov_b32 v32, 0\n\t"
        "v_mov_b32 v33, 0\n\t"
        "v_mov_b32 v34, 0\n\t"
        "v_mov_b32 v35, 0\n\t"
        "v_mov_b32 v36, 0\n\t"
        "v_mov_b32 v37, 0\n\t"
        "v_mov_b32 v38, 0\n\t"
        "v_mov_b32 v39, 0\n\t"
        "v_mov_b32 v40, 0\n\t"
        "v_mov_b32 v41, 0\n\t"
        "v_mov_b32 v42, 0\n\t"
        "v_mov_b32 v43, 0\n\t"
        "v_mov_b32 v44, 0\n\t"
        "v_mov_b32 v45, 0\n\t"
        "v_mov_b32 v46, 0\n\t"
        "v_mov_b32 v47, 0\n\t"
        "v_mov_b32 v48, 0\n\t"
        "v_mov_b32 v49, 0\n\t"
        "v_mov_b32 v50, 0\n\t"
        "v_mov_b32 v51, 0\n\t"
        "v_mov_b32 v52, 0\n\t"
        "v_mov_b32 v53, 0\n\t"
        "v_mov_b32 v54, 0\n\t"
        "v_mov_b32 v55, 0\n\t"
        "v_mov_b32 v56, 0\n\t"
        "v_mov_b32 v57, 0\n\t"
        "v_mov_b32 v58, 0\n\t"
        "v_mov_b32 v59, 0\n\t"
        "v_mov_b32 v60, 0\n\t"
        "v_mov_b32 v61, 0\n\t"
        "v_mov_b32 v62, 0\n\t"
        "v_mov_b32 v63, 0\n\t"
        "v_mov_b32 v64, 0\n\t"
        "v_mov_b32 v65, 0\n\t"
        "v_mov_b32 v66, 0\n\t"
        "v_mov_b32 v67, 0\n\t"
        "v_mov_b32 v68, 0\n\t"
        "v_mov_b32 v69, 0\n\t"
        "v_mov_b32 v70, 0\n\t"
        "v_mov_b32 v71, 0\n\t"
        "v_mov_b32 v72, 0\n\t"
        "v_mov_b32 v73, 0\n\t"
        "v_mov_b32 v74, 0\n\t"
        "v_mov_b32 v75, 0\n\t"
        "v_mov_b32 v76, 0\n\t"
        "v_mov_b32 v77, 0\n\t"
        "v_mov_b32 v78, 0\n\t"
        "v_mov_b32 v79, 0\n\t"
        "v_mov_b32 v80, 0\n\t"
        "v_mov_b32 v81, 0\n\t"
        "v_mov_b32 v82, 0\n\t"
        "v_mov_b32 v83, 0\n\t"
        "v_mov_b32 v84, 0\n\t"
        "v_mov_b32 v85, 0\n\t"
        "v_mov_b32 v86, 0\n\t"
        "v_mov_b32 v87, 0\n\t"
        "v_mov_b32 v88, 0\n\t"
        "v_mov_b32 v89, 0\n\t"
        "v_mov_b32 v90, 0\n\t"
        "v_mov_b32 v91, 0\n\t"
        "v_mov_b32 v92, 0\n\t"
        "v_mov_b32 v93, 0\n\t"
        "v_mov_b32 v94, 0\n\t"
        "v_mov_b32 v95, 0\n\t"
        "v_mov_b32 v96, 0\n\t"
        "v_mov_b32 v97, 0\n\t"
        "v_mov_b32 v98, 0\n\t"
        "v_mov_b32 v99, 0\n\t"
        "v_mov_b32 v100, 0\n\t"
        "v_mov_b32 v101, 0\n\t"
        "v_mov_b32 v102, 0\n\t"
        "v_mov_b32 v103, 0\n\t"
        "v_mov_b32 v104, 0\n\t"
        "v_mov_b32 v105, 0\n\t"
        "v_mov_b32 v106, 0\n\t"
        "v_mov_b32 v107, 0\n\t"
        "v_mov_b32 v108, 0\n\t"
        "v_mov_b32 v109, 0\n\t"
        "v_mov_b32 v110, 0\n\t"
        "v_mov_b32 v111, 0\n\t"
        "s_mov_b32 s47, 4294873088\n\t"
        "s_mov_b32 s48, 47104\n\t"
        "s_mul_i32 s84, s36, 14336\n\t"
        "s_add_u32 s85, s84, 114688\n\t"
        "s_waitcnt vmcnt(9)\n\t"
        "v_lshlrev_b32 v152, 16, v144\n\t"
        "v_and_b32 v153, 0xffff0000, v144\n\t"
        "v_lshlrev_b32 v156, 16, v146\n\t"
        "v_and_b32 v157, 0xffff0000, v146\n\t"
        "v_lshlrev_b32 v154, 16, v145\n\t"
        "v_and_b32 v155, 0xffff0000, v145\n\t"
        "v_lshlrev_b32 v158, 16, v147\n\t"
        "v_and_b32 v159, 0xffff0000, v147\n\t"
        "v_lshlrev_b32 v160, 16, v148\n\t"
        "v_and_b32 v161, 0xffff0000, v148\n\t"
        "v_lshlrev_b32 v162, 16, v149\n\t"
        "v_and_b32 v163, 0xffff0000, v149\n\t"
        "v_lshlrev_b32 v164, 16, v150\n\t"
        "v_and_b32 v165, 0xffff0000, v150\n\t"
        "v_lshlrev_b32 v166, 16, v151\n\t"
        "v_and_b32 v167, 0xffff0000, v151\n\t"
        "v_mul_f32_e64 v156, -v156, v152\n\t"
        "v_mul_f32_e64 v157, -v157, v153\n\t"
        "v_mul_f32_e64 v158, -v158, v154\n\t"
        "v_mul_f32_e64 v159, -v159, v155\n\t"
        "v_cvt_f32_i32 v168, v112\n\t"
        "v_cvt_f32_i32 v169, v116\n\t"
        "v_cvt_f32_i32 v170, v120\n\t"
        "v_cvt_f32_i32 v171, v124\n\t"
        "v_cvt_f32_i32 v172, v128\n\t"
        "v_cvt_f32_i32 v173, v132\n\t"
        "v_cvt_f32_i32 v174, v136\n\t"
        "v_cvt_f32_i32 v175, v140\n\t"
        "v_fma_f32 v168, v168, v152, v156\n\t"
        "v_fma_f32 v169, v169, v152, v156\n\t"
        "v_fma_f32 v170, v170, v152, v156\n\t"
        "v_fma_f32 v171, v171, v152, v156\n\t"
        "v_fma_f32 v172, v172, v152, v156\n\t"
        "v_fma_f32 v173, v173, v152, v156\n\t"
        "v_fma_f32 v174, v174, v152, v156\n\t"
        "v_fma_f32 v175, v175, v152, v156\n\t"
        "v_mul_f32 v168, v168, v160\n\t"
        "v_mul_f32 v169, v169, v161\n\t"
        "v_mul_f32 v170, v170, v162\n\t"
        "v_mul_f32 v171, v171, v163\n\t"
        "v_mul_f32 v172, v172, v164\n\t"
        "v_mul_f32 v173, v173, v165\n\t"
        "v_mul_f32 v174, v174, v166\n\t"
        "v_mul_f32 v175, v175, v167\n\t"
        "v_cvt_pk_bf16_f32 v176, v168, v169\n\t"
        "v_cvt_pk_bf16_f32 v177, v170, v171\n\t"
        "v_cvt_pk_bf16_f32 v178, v172, v173\n\t"
        "v_cvt_pk_bf16_f32 v179, v174, v175\n\t"
        "ds_write_b128 v242, v[176:179] offset:0\n\t"
        "v_cvt_f32_i32 v168, v113\n\t"
        "v_cvt_f32_i32 v169, v117\n\t"
        "v_cvt_f32_i32 v170, v121\n\t"
        "v_cvt_f32_i32 v171, v125\n\t"
        "v_cvt_f32_i32 v172, v129\n\t"
        "v_cvt_f32_i32 v173, v133\n\t"
        "v_cvt_f32_i32 v174, v137\n\t"
        "v_cvt_f32_i32 v175, v141\n\t"
        "v_fma_f32 v168, v168, v153, v157\n\t"
        "v_fma_f32 v169, v169, v153, v157\n\t"
        "v_fma_f32 v170, v170, v153, v157\n\t"
        "v_fma_f32 v171, v171, v153, v157\n\t"
        "v_fma_f32 v172, v172, v153, v157\n\t"
        "v_fma_f32 v173, v173, v153, v157\n\t"
        "v_fma_f32 v174, v174, v153, v157\n\t"
        "v_fma_f32 v175, v175, v153, v157\n\t"
        "v_mul_f32 v168, v168, v160\n\t"
        "v_mul_f32 v169, v169, v161\n\t"
        "v_mul_f32 v170, v170, v162\n\t"
        "v_mul_f32 v171, v171, v163\n\t"
        "v_mul_f32 v172, v172, v164\n\t"
        "v_mul_f32 v173, v173, v165\n\t"
        "v_mul_f32 v174, v174, v166\n\t"
        "v_mul_f32 v175, v175, v167\n\t"
        "v_cvt_pk_bf16_f32 v180, v168, v169\n\t"
        "v_cvt_pk_bf16_f32 v181, v170, v171\n\t"
        "v_cvt_pk_bf16_f32 v182, v172, v173\n\t"
        "v_cvt_pk_bf16_f32 v183, v174, v175\n\t"
        "ds_write_b128 v242, v[180:183] offset:128\n\t"
        "v_cvt_f32_i32 v168, v114\n\t"
        "v_cvt_f32_i32 v169, v118\n\t"
        "v_cvt_f32_i32 v170, v122\n\t"
        "v_cvt_f32_i32 v171, v126\n\t"
        "v_cvt_f32_i32 v172, v130\n\t"
        "v_cvt_f32_i32 v173, v134\n\t"
        "v_cvt_f32_i32 v174, v138\n\t"
        "v_cvt_f32_i32 v175, v142\n\t"
        "v_fma_f32 v168, v168, v154, v158\n\t"
        "v_fma_f32 v169, v169, v154, v158\n\t"
        "v_fma_f32 v170, v170, v154, v158\n\t"
        "v_fma_f32 v171, v171, v154, v158\n\t"
        "v_fma_f32 v172, v172, v154, v158\n\t"
        "v_fma_f32 v173, v173, v154, v158\n\t"
        "v_fma_f32 v174, v174, v154, v158\n\t"
        "v_fma_f32 v175, v175, v154, v158\n\t"
        "v_mul_f32 v168, v168, v160\n\t"
        "v_mul_f32 v169, v169, v161\n\t"
        "v_mul_f32 v170, v170, v162\n\t"
        "v_mul_f32 v171, v171, v163\n\t"
        "v_mul_f32 v172, v172, v164\n\t"
        "v_mul_f32 v173, v173, v165\n\t"
        "v_mul_f32 v174, v174, v166\n\t"
        "v_mul_f32 v175, v175, v167\n\t"
        "v_cvt_pk_bf16_f32 v184, v168, v169\n\t"
        "v_cvt_pk_bf16_f32 v185, v170, v171\n\t"
        "v_cvt_pk_bf16_f32 v186, v172, v173\n\t"
        "v_cvt_pk_bf16_f32 v187, v174, v175\n\t"
        "ds_write_b128 v243, v[184:187] offset:0\n\t"
        "v_cvt_f32_i32 v168, v115\n\t"
        "v_cvt_f32_i32 v169, v119\n\t"
        "v_cvt_f32_i32 v170, v123\n\t"
        "v_cvt_f32_i32 v171, v127\n\t"
        "v_cvt_f32_i32 v172, v131\n\t"
        "v_cvt_f32_i32 v173, v135\n\t"
        "v_cvt_f32_i32 v174, v139\n\t"
        "v_cvt_f32_i32 v175, v143\n\t"
        "v_fma_f32 v168, v168, v155, v159\n\t"
        "v_fma_f32 v169, v169, v155, v159\n\t"
        "v_fma_f32 v170, v170, v155, v159\n\t"
        "v_fma_f32 v171, v171, v155, v159\n\t"
        "v_fma_f32 v172, v172, v155, v159\n\t"
        "v_fma_f32 v173, v173, v155, v159\n\t"
        "v_fma_f32 v174, v174, v155, v159\n\t"
        "v_fma_f32 v175, v175, v155, v159\n\t"
        "v_mul_f32 v168, v168, v160\n\t"
        "v_mul_f32 v169, v169, v161\n\t"
        "v_mul_f32 v170, v170, v162\n\t"
        "v_mul_f32 v171, v171, v163\n\t"
        "v_mul_f32 v172, v172, v164\n\t"
        "v_mul_f32 v173, v173, v165\n\t"
        "v_mul_f32 v174, v174, v166\n\t"
        "v_mul_f32 v175, v175, v167\n\t"
        "v_cvt_pk_bf16_f32 v188, v168, v169\n\t"
        "v_cvt_pk_bf16_f32 v189, v170, v171\n\t"
        "v_cvt_pk_bf16_f32 v190, v172, v173\n\t"
        "v_cvt_pk_bf16_f32 v191, v174, v175\n\t"
        "ds_write_b128 v243, v[188:191] offset:128\n\t"
        "s_waitcnt vmcnt(0) lgkmcnt(0)\n\t"
        "v_add_co_u32 v248, vcc, 1, v198\n\t"
        "s_nop 1\n\t"
        "v_addc_co_u32 v249, vcc, 0, v199, vcc\n\t"
        "s_barrier\n\t"
        "ds_read_b128 v[144:147], v241 offset:0\n\t"
        "ds_read_b128 v[148:151], v241 offset:1024\n\t"
        "ds_read_b128 v[152:155], v241 offset:2048\n\t"
        "ds_read_b128 v[156:159], v241 offset:3072\n\t"
        "s_waitcnt lgkmcnt(3)\n\t"
        "buffer_store_dwordx4 v[144:147], v240, s[24:27], s84 offen offset:0 sc1\n\t"
        "s_waitcnt lgkmcnt(2)\n\t"
        "buffer_store_dwordx4 v[148:151], v240, s[24:27], s84 offen offset:1024 sc1\n\t"
        "s_waitcnt lgkmcnt(1)\n\t"
        "buffer_store_dwordx4 v[152:155], v240, s[24:27], s84 offen offset:2048 sc1\n\t"
        "s_waitcnt lgkmcnt(0)\n\t"
        "s_mov_b32 exec_hi, 0\n\t"
        "buffer_store_dwordx4 v[156:159], v240, s[24:27], s84 offen offset:3072 sc1\n\t"
        "s_mov_b32 exec_hi, -1\n\t"
        "ds_read_b128 v[112:115], v232 offset:0\n\t"
        "ds_read_b128 v[176:179], v234 offset:32768\n\t"
        "ds_read_b128 v[128:131], v233 offset:0\n\t"
        "ds_read_b128 v[180:183], v235 offset:32768\n\t"
        "ds_read_b128 v[116:119], v232 offset:2048\n\t"
        "ds_read_b128 v[132:135], v233 offset:2048\n\t"
        "ds_read_b128 v[120:123], v232 offset:4096\n\t"
        "ds_read_b128 v[136:139], v233 offset:4096\n\t"
        "ds_read_b128 v[124:127], v232 offset:6144\n\t"
        "ds_read_b128 v[140:143], v233 offset:6144\n\t"
        "ds_read_b128 v[184:187], v234 offset:34816\n\t"
        "ds_read_b128 v[188:191], v235 offset:34816\n\t"
        "ds_read_b128 v[192:195], v234 offset:36864\n\t"
        "ds_read_b128 v[196:199], v235 offset:36864\n\t"
        "ds_read_b128 v[200:203], v234 offset:38912\n\t"
        "ds_read_b128 v[204:207], v235 offset:38912\n\t"
        "v_add_u32 v241, 47104, v241\n\t"
        "s_waitcnt lgkmcnt(0)\n\t"
        "v_mfma_f32_16x16x32_bf16 v[0:3], v[176:179], v[112:115], v[0:3]\n\t"
        "ds_read_b128 v[208:211], v234 offset:40960\n\t"
        "v_mfma_f32_16x16x32_bf16 v[0:3], v[180:183], v[128:131], v[0:3]\n\t"
        "ds_read_b128 v[212:215], v235 offset:40960\n\t"
        "v_mfma_f32_16x16x32_bf16 v[28:31], v[176:179], v[116:119], v[28:31]\n\t"
        "ds_read_b128 v[216:219], v234 offset:43008\n\t"
        "v_mfma_f32_16x16x32_bf16 v[28:31], v[180:183], v[132:135], v[28:31]\n\t"
        "ds_read_b128 v[220:223], v235 offset:43008\n\t"
        "v_mfma_f32_16x16x32_bf16 v[56:59], v[176:179], v[120:123], v[56:59]\n\t"
        "ds_read_b128 v[224:227], v234 offset:45056\n\t"
        "v_mfma_f32_16x16x32_bf16 v[56:59], v[180:183], v[136:139], v[56:59]\n\t"
        "ds_read_b128 v[228:231], v235 offset:45056\n\t"
        "v_mfma_f32_16x16x32_bf16 v[84:87], v[176:179], v[124:127], v[84:87]\n\t"
        "v_mfma_f32_16x16x32_bf16 v[84:87], v[180:183], v[140:143], v[84:87]\n\t"
        "v_mfma_f32_16x16x32_bf16 v[88:91], v[184:187], v[124:127], v[88:91]\n\t"
        "v_mfma_f32_16x16x32_bf16 v[88:91], v[188:191], v[140:143], v[88:91]\n\t"
        "v_mfma_f32_16x16x32_bf16 v[60:63], v[184:187], v[120:123], v[60:63]\n\t"
        "v_mfma_f32_16x16x32_bf16 v[60:63], v[188:191], v[136:139], v[60:63]\n\t"
        "v_mfma_f32_16x16x32_bf16 v[32:35], v[184:187], v[116:119], v[32:35]\n\t"
        "v_add_u32 v232, s48, v232\n\t"
        "v_mfma_f32_16x16x32_bf16 v[32:35], v[188:191], v[132:135], v[32:35]\n\t"
        "v_add_u32 v233, s48, v233\n\t"
        "v_mfma_f32_16x16x32_bf16 v[4:7], v[184:187], v[112:115], v[4:7]\n\t"
        "v_add_u32 v234, s48, v234\n\t"
        "v_mfma_f32_16x16x32_bf16 v[4:7], v[188:191], v[128:131], v[4:7]\n\t"
        "v_add_u32 v235, s48, v235\n\t"
        "v_mfma_f32_16x16x32_bf16 v[8:11], v[192:195], v[112:115], v[8:11]\n\t"
        "v_mfma_f32_16x16x32_bf16 v[8:11], v[196:199], v[128:131], v[8:11]\n\t"
        "v_mfma_f32_16x16x32_bf16 v[36:39], v[192:195], v[116:119], v[36:39]\n\t"
        "v_mfma_f32_16x16x32_bf16 v[36:39], v[196:199], v[132:135], v[36:39]\n\t"
        "v_mfma_f32_16x16x32_bf16 v[64:67], v[192:195], v[120:123], v[64:67]\n\t"
        "v_mfma_f32_16x16x32_bf16 v[64:67], v[196:199], v[136:139], v[64:67]\n\t"
        "v_mfma_f32_16x16x32_bf16 v[92:95], v[192:195], v[124:127], v[92:95]\n\t"
        "v_mfma_f32_16x16x32_bf16 v[92:95], v[196:199], v[140:143], v[92:95]\n\t"
        "v_mfma_f32_16x16x32_bf16 v[96:99], v[200:203], v[124:127], v[96:99]\n\t"
        "v_mfma_f32_16x16x32_bf16 v[96:99], v[204:207], v[140:143], v[96:99]\n\t"
        "v_mfma_f32_16x16x32_bf16 v[68:71], v[200:203], v[120:123], v[68:71]\n\t"
        "v_mfma_f32_16x16x32_bf16 v[68:71], v[204:207], v[136:139], v[68:71]\n\t"
        "v_mfma_f32_16x16x32_bf16 v[40:43], v[200:203], v[116:119], v[40:43]\n\t"
        "v_mfma_f32_16x16x32_bf16 v[40:43], v[204:207], v[132:135], v[40:43]\n\t"
        "v_mfma_f32_16x16x32_bf16 v[12:15], v[200:203], v[112:115], v[12:15]\n\t"
        "v_mfma_f32_16x16x32_bf16 v[12:15], v[204:207], v[128:131], v[12:15]\n\t"
        "s_waitcnt lgkmcnt(0)\n\t"
        "v_mfma_f32_16x16x32_bf16 v[16:19], v[208:211], v[112:115], v[16:19]\n\t"
        "v_mfma_f32_16x16x32_bf16 v[16:19], v[212:215], v[128:131], v[16:19]\n\t"
        "v_mfma_f32_16x16x32_bf16 v[44:47], v[208:211], v[116:119], v[44:47]\n\t"
        "s_waitcnt vmcnt(0)\n\t"
        "s_mov_b64 exec, 1\n\t"
        "buffer_store_dwordx2 v[248:249], v250, s[76:79], s90 offen offset:0 sc1\n\t"
        "s_mov_b64 exec, -1\n\t"
        "s_barrier\n\t"
        "v_mfma_f32_16x16x32_bf16 v[44:47], v[212:215], v[132:135], v[44:47]\n\t"
        "ds_read_b128 v[144:147], v232 offset:0\n\t"
        "v_mfma_f32_16x16x32_bf16 v[72:75], v[208:211], v[120:123], v[72:75]\n\t"
        "ds_read_b128 v[176:179], v234 offset:32768\n\t"
        "v_mfma_f32_16x16x32_bf16 v[72:75], v[212:215], v[136:139], v[72:75]\n\t"
        "ds_read_b128 v[160:163], v233 offset:0\n\t"
        "v_mfma_f32_16x16x32_bf16 v[100:103], v[208:211], v[124:127], v[100:103]\n\t"
        "ds_read_b128 v[180:183], v235 offset:32768\n\t"
        "v_mfma_f32_16x16x32_bf16 v[100:103], v[212:215], v[140:143], v[100:103]\n\t"
        "ds_read_b128 v[148:151], v232 offset:2048\n\t"
        "v_mfma_f32_16x16x32_bf16 v[104:107], v[216:219], v[124:127], v[104:107]\n\t"
        "ds_read_b128 v[164:167], v233 offset:2048\n\t"
        "v_mfma_f32_16x16x32_bf16 v[104:107], v[220:223], v[140:143], v[104:107]\n\t"
        "ds_read_b128 v[152:155], v232 offset:4096\n\t"
        "v_mfma_f32_16x16x32_bf16 v[76:79], v[216:219], v[120:123], v[76:79]\n\t"
        "ds_read_b128 v[168:171], v233 offset:4096\n\t"
        "v_mfma_f32_16x16x32_bf16 v[76:79], v[220:223], v[136:139], v[76:79]\n\t"
        "ds_read_b128 v[156:159], v232 offset:6144\n\t"
        "v_mfma_f32_16x16x32_bf16 v[48:51], v[216:219], v[116:119], v[48:51]\n\t"
        "ds_read_b128 v[172:175], v233 offset:6144\n\t"
        "v_mfma_f32_16x16x32_bf16 v[48:51], v[220:223], v[132:135], v[48:51]\n\t"
        "ds_read_b128 v[184:187], v234 offset:34816\n\t"
        "v_mfma_f32_16x16x32_bf16 v[20:23], v[216:219], v[112:115], v[20:23]\n\t"
        "ds_read_b128 v[188:191], v235 offset:34816\n\t"
        "v_mfma_f32_16x16x32_bf16 v[20:23], v[220:223], v[128:131], v[20:23]\n\t"
        "ds_read_b128 v[192:195], v234 offset:36864\n\t"
        "v_mfma_f32_16x16x32_bf16 v[24:27], v[224:227], v[112:115], v[24:27]\n\t"
        "ds_read_b128 v[196:199], v235 offset:36864\n\t"
        "v_mfma_f32_16x16x32_bf16 v[24:27], v[228:231], v[128:131], v[24:27]\n\t"
        "ds_read_b128 v[200:203], v234 offset:38912\n\t"
        "v_mfma_f32_16x16x32_bf16 v[52:55], v[224:227], v[116:119], v[52:55]\n\t"
        "ds_read_b128 v[204:207], v235 offset:38912\n\t"
        "v_mfma_f32_16x16x32_bf16 v[52:55], v[228:231], v[132:135], v[52:55]\n\t"
        "v_mfma_f32_16x16x32_bf16 v[80:83], v[224:227], v[120:123], v[80:83]\n\t"
        "v_mfma_f32_16x16x32_bf16 v[80:83], v[228:231], v[136:139], v[80:83]\n\t"
        "v_mfma_f32_16x16x32_bf16 v[108:111], v[224:227], v[124:127], v[108:111]\n\t"
        "v_mfma_f32_16x16x32_bf16 v[108:111], v[228:231], v[140:143], v[108:111]\n\t"
        "s_waitcnt lgkmcnt(0)\n\t"
        "v_mfma_f32_16x16x32_bf16 v[0:3], v[176:179], v[144:147], v[0:3]\n\t"
        "ds_read_b128 v[112:115], v241 offset:0\n\t"
        "v_mfma_f32_16x16x32_bf16 v[0:3], v[180:183], v[160:163], v[0:3]\n\t"
        "ds_read_b128 v[116:119], v241 offset:1024\n\t"
        "v_mfma_f32_16x16x32_bf16 v[28:31], v[176:179], v[148:151], v[28:31]\n\t"
        "ds_read_b128 v[120:123], v241 offset:2048\n\t"
        "v_mfma_f32_16x16x32_bf16 v[28:31], v[180:183], v[164:167], v[28:31]\n\t"
        "ds_read_b128 v[124:127], v241 offset:3072\n\t"
        "v_mfma_f32_16x16x32_bf16 v[56:59], v[176:179], v[152:155], v[56:59]\n\t"
        "ds_read_b128 v[208:211], v234 offset:40960\n\t"
        "v_mfma_f32_16x16x32_bf16 v[56:59], v[180:183], v[168:171], v[56:59]\n\t"
        "ds_read_b128 v[212:215], v235 offset:40960\n\t"
        "v_mfma_f32_16x16x32_bf16 v[84:87], v[176:179], v[156:159], v[84:87]\n\t"
        "ds_read_b128 v[216:219], v234 offset:43008\n\t"
        "v_mfma_f32_16x16x32_bf16 v[84:87], v[180:183], v[172:175], v[84:87]\n\t"
        "ds_read_b128 v[220:223], v235 offset:43008\n\t"
        "v_mfma_f32_16x16x32_bf16 v[88:91], v[184:187], v[156:159], v[88:91]\n\t"
        "ds_read_b128 v[224:227], v234 offset:45056\n\t"
        "v_mfma_f32_16x16x32_bf16 v[88:91], v[188:191], v[172:175], v[88:91]\n\t"
        "ds_read_b128 v[228:231], v235 offset:45056\n\t"
        "v_mfma_f32_16x16x32_bf16 v[60:63], v[184:187], v[152:155], v[60:63]\n\t"
        "v_mfma_f32_16x16x32_bf16 v[60:63], v[188:191], v[168:171], v[60:63]\n\t"
        "v_mfma_f32_16x16x32_bf16 v[32:35], v[184:187], v[148:151], v[32:35]\n\t"
        "v_add_u32 v232, s48, v232\n\t"
        "s_waitcnt lgkmcnt(6)\n\t"
        "buffer_store_dwordx4 v[112:115], v240, s[24:27], s85 offen offset:0 sc1\n\t"
        "v_mfma_f32_16x16x32_bf16 v[32:35], v[188:191], v[164:167], v[32:35]\n\t"
        "v_add_u32 v233, s48, v233\n\t"
        "v_mfma_f32_16x16x32_bf16 v[4:7], v[184:187], v[144:147], v[4:7]\n\t"
        "v_add_u32 v234, s48, v234\n\t"
        "buffer_store_dwordx4 v[116:119], v240, s[24:27], s85 offen offset:1024 sc1\n\t"
        "v_mfma_f32_16x16x32_bf16 v[4:7], v[188:191], v[160:163], v[4:7]\n\t"
        "v_add_u32 v235, s48, v235\n\t"
        "v_mfma_f32_16x16x32_bf16 v[8:11], v[192:195], v[144:147], v[8:11]\n\t"
        "buffer_store_dwordx4 v[120:123], v240, s[24:27], s85 offen offset:2048 sc1\n\t"
        "v_mfma_f32_16x16x32_bf16 v[8:11], v[196:199], v[160:163], v[8:11]\n\t"
        "v_mfma_f32_16x16x32_bf16 v[36:39], v[192:195], v[148:151], v[36:39]\n\t"
        "s_mov_b32 exec_hi, 0\n\t"
        "buffer_store_dwordx4 v[124:127], v240, s[24:27], s85 offen offset:3072 sc1\n\t"
        "s_mov_b32 exec_hi, -1\n\t"
        "v_mfma_f32_16x16x32_bf16 v[36:39], v[196:199], v[164:167], v[36:39]\n\t"
        "v_mfma_f32_16x16x32_bf16 v[64:67], v[192:195], v[152:155], v[64:67]\n\t"
        "v_mfma_f32_16x16x32_bf16 v[64:67], v[196:199], v[168:171], v[64:67]\n\t"
        "v_mfma_f32_16x16x32_bf16 v[92:95], v[192:195], v[156:159], v[92:95]\n\t"
        "v_mfma_f32_16x16x32_bf16 v[92:95], v[196:199], v[172:175], v[92:95]\n\t"
        "v_mfma_f32_16x16x32_bf16 v[96:99], v[200:203], v[156:159], v[96:99]\n\t"
        "v_mfma_f32_16x16x32_bf16 v[96:99], v[204:207], v[172:175], v[96:99]\n\t"
        "v_mfma_f32_16x16x32_bf16 v[68:71], v[200:203], v[152:155], v[68:71]\n\t"
        "v_mfma_f32_16x16x32_bf16 v[68:71], v[204:207], v[168:171], v[68:71]\n\t"
        "v_mfma_f32_16x16x32_bf16 v[40:43], v[200:203], v[148:151], v[40:43]\n\t"
        "v_mfma_f32_16x16x32_bf16 v[40:43], v[204:207], v[164:167], v[40:43]\n\t"
        "v_mfma_f32_16x16x32_bf16 v[12:15], v[200:203], v[144:147], v[12:15]\n\t"
        "v_mfma_f32_16x16x32_bf16 v[12:15], v[204:207], v[160:163], v[12:15]\n\t"
        "s_waitcnt lgkmcnt(0)\n\t"
        "v_mfma_f32_16x16x32_bf16 v[16:19], v[208:211], v[144:147], v[16:19]\n\t"
        "v_mfma_f32_16x16x32_bf16 v[16:19], v[212:215], v[160:163], v[16:19]\n\t"
        "v_mfma_f32_16x16x32_bf16 v[44:47], v[208:211], v[148:151], v[44:47]\n\t"
        "s_barrier\n\t"
        "v_mfma_f32_16x16x32_bf16 v[44:47], v[212:215], v[164:167], v[44:47]\n\t"
        "ds_read_b128 v[112:115], v232 offset:0\n\t"
        "v_mfma_f32_16x16x32_bf16 v[72:75], v[208:211], v[152:155], v[72:75]\n\t"
        "ds_read_b128 v[176:179], v234 offset:32768\n\t"
        "v_mfma_f32_16x16x32_bf16 v[72:75], v[212:215], v[168:171], v[72:75]\n\t"
        "ds_read_b128 v[128:131], v233 offset:0\n\t"
        "v_mfma_f32_16x16x32_bf16 v[100:103], v[208:211], v[156:159], v[100:103]\n\t"
        "ds_read_b128 v[180:183], v235 offset:32768\n\t"
        "v_mfma_f32_16x16x32_bf16 v[100:103], v[212:215], v[172:175], v[100:103]\n\t"
        "ds_read_b128 v[116:119], v232 offset:2048\n\t"
        "v_mfma_f32_16x16x32_bf16 v[104:107], v[216:219], v[156:159], v[104:107]\n\t"
        "ds_read_b128 v[132:135], v233 offset:2048\n\t"
        "v_mfma_f32_16x16x32_bf16 v[104:107], v[220:223], v[172:175], v[104:107]\n\t"
        "ds_read_b128 v[120:123], v232 offset:4096\n\t"
        "v_mfma_f32_16x16x32_bf16 v[76:79], v[216:219], v[152:155], v[76:79]\n\t"
        "ds_read_b128 v[136:139], v233 offset:4096\n\t"
        "v_mfma_f32_16x16x32_bf16 v[76:79], v[220:223], v[168:171], v[76:79]\n\t"
        "ds_read_b128 v[124:127], v232 offset:6144\n\t"
        "v_mfma_f32_16x16x32_bf16 v[48:51], v[216:219], v[148:151], v[48:51]\n\t"
        "ds_read_b128 v[140:143], v233 offset:6144\n\t"
        "v_mfma_f32_16x16x32_bf16 v[48:51], v[220:223], v[164:167], v[48:51]\n\t"
        "ds_read_b128 v[184:187], v234 offset:34816\n\t"
        "v_mfma_f32_16x16x32_bf16 v[20:23], v[216:219], v[144:147], v[20:23]\n\t"
        "ds_read_b128 v[188:191], v235 offset:34816\n\t"
        "v_mfma_f32_16x16x32_bf16 v[20:23], v[220:223], v[160:163], v[20:23]\n\t"
        "ds_read_b128 v[192:195], v234 offset:36864\n\t"
        "v_mfma_f32_16x16x32_bf16 v[24:27], v[224:227], v[144:147], v[24:27]\n\t"
        "ds_read_b128 v[196:199], v235 offset:36864\n\t"
        "v_mfma_f32_16x16x32_bf16 v[24:27], v[228:231], v[160:163], v[24:27]\n\t"
        "ds_read_b128 v[200:203], v234 offset:38912\n\t"
        "v_mfma_f32_16x16x32_bf16 v[52:55], v[224:227], v[148:151], v[52:55]\n\t"
        "ds_read_b128 v[204:207], v235 offset:38912\n\t"
        "v_mfma_f32_16x16x32_bf16 v[52:55], v[228:231], v[164:167], v[52:55]\n\t"
        "v_mfma_f32_16x16x32_bf16 v[80:83], v[224:227], v[152:155], v[80:83]\n\t"
        "v_mfma_f32_16x16x32_bf16 v[80:83], v[228:231], v[168:171], v[80:83]\n\t"
        "v_mfma_f32_16x16x32_bf16 v[108:111], v[224:227], v[156:159], v[108:111]\n\t"
        "v_mfma_f32_16x16x32_bf16 v[108:111], v[228:231], v[172:175], v[108:111]\n\t"
        "s_waitcnt lgkmcnt(0)\n\t"
        "v_mfma_f32_16x16x32_bf16 v[0:3], v[176:179], v[112:115], v[0:3]\n\t"
        "ds_read_b128 v[208:211], v234 offset:40960\n\t"
        "v_mfma_f32_16x16x32_bf16 v[0:3], v[180:183], v[128:131], v[0:3]\n\t"
        "ds_read_b128 v[212:215], v235 offset:40960\n\t"
        "v_mfma_f32_16x16x32_bf16 v[28:31], v[176:179], v[116:119], v[28:31]\n\t"
        "ds_read_b128 v[216:219], v234 offset:43008\n\t"
        "v_mfma_f32_16x16x32_bf16 v[28:31], v[180:183], v[132:135], v[28:31]\n\t"
        "ds_read_b128 v[220:223], v235 offset:43008\n\t"
        "v_mfma_f32_16x16x32_bf16 v[56:59], v[176:179], v[120:123], v[56:59]\n\t"
        "ds_read_b128 v[224:227], v234 offset:45056\n\t"
        "v_mfma_f32_16x16x32_bf16 v[56:59], v[180:183], v[136:139], v[56:59]\n\t"
        "ds_read_b128 v[228:231], v235 offset:45056\n\t"
        "v_mfma_f32_16x16x32_bf16 v[84:87], v[176:179], v[124:127], v[84:87]\n\t"
        "v_mfma_f32_16x16x32_bf16 v[84:87], v[180:183], v[140:143], v[84:87]\n\t"
        "v_mfma_f32_16x16x32_bf16 v[88:91], v[184:187], v[124:127], v[88:91]\n\t"
        "v_mfma_f32_16x16x32_bf16 v[88:91], v[188:191], v[140:143], v[88:91]\n\t"
        "v_mfma_f32_16x16x32_bf16 v[60:63], v[184:187], v[120:123], v[60:63]\n\t"
        "v_mfma_f32_16x16x32_bf16 v[60:63], v[188:191], v[136:139], v[60:63]\n\t"
        "v_mfma_f32_16x16x32_bf16 v[32:35], v[184:187], v[116:119], v[32:35]\n\t"
        "v_add_u32 v232, s47, v232\n\t"
        "v_mfma_f32_16x16x32_bf16 v[32:35], v[188:191], v[132:135], v[32:35]\n\t"
        "v_add_u32 v233, s47, v233\n\t"
        "v_mfma_f32_16x16x32_bf16 v[4:7], v[184:187], v[112:115], v[4:7]\n\t"
        "v_add_u32 v234, s47, v234\n\t"
        "v_mfma_f32_16x16x32_bf16 v[4:7], v[188:191], v[128:131], v[4:7]\n\t"
        "v_add_u32 v235, s47, v235\n\t"
        "v_mfma_f32_16x16x32_bf16 v[8:11], v[192:195], v[112:115], v[8:11]\n\t"
        "v_mfma_f32_16x16x32_bf16 v[8:11], v[196:199], v[128:131], v[8:11]\n\t"
        "v_mfma_f32_16x16x32_bf16 v[36:39], v[192:195], v[116:119], v[36:39]\n\t"
        "v_mfma_f32_16x16x32_bf16 v[36:39], v[196:199], v[132:135], v[36:39]\n\t"
        "v_mfma_f32_16x16x32_bf16 v[64:67], v[192:195], v[120:123], v[64:67]\n\t"
        "v_mfma_f32_16x16x32_bf16 v[64:67], v[196:199], v[136:139], v[64:67]\n\t"
        "v_mfma_f32_16x16x32_bf16 v[92:95], v[192:195], v[124:127], v[92:95]\n\t"
        "v_mfma_f32_16x16x32_bf16 v[92:95], v[196:199], v[140:143], v[92:95]\n\t"
        "v_mfma_f32_16x16x32_bf16 v[96:99], v[200:203], v[124:127], v[96:99]\n\t"
        "v_mfma_f32_16x16x32_bf16 v[96:99], v[204:207], v[140:143], v[96:99]\n\t"
        "v_mfma_f32_16x16x32_bf16 v[68:71], v[200:203], v[120:123], v[68:71]\n\t"
        "v_mfma_f32_16x16x32_bf16 v[68:71], v[204:207], v[136:139], v[68:71]\n\t"
        "v_mfma_f32_16x16x32_bf16 v[40:43], v[200:203], v[116:119], v[40:43]\n\t"
        "v_mfma_f32_16x16x32_bf16 v[40:43], v[204:207], v[132:135], v[40:43]\n\t"
        "v_mfma_f32_16x16x32_bf16 v[12:15], v[200:203], v[112:115], v[12:15]\n\t"
        "v_mfma_f32_16x16x32_bf16 v[12:15], v[204:207], v[128:131], v[12:15]\n\t"
        "s_waitcnt lgkmcnt(0)\n\t"
        "v_mfma_f32_16x16x32_bf16 v[16:19], v[208:211], v[112:115], v[16:19]\n\t"
        "v_mfma_f32_16x16x32_bf16 v[16:19], v[212:215], v[128:131], v[16:19]\n\t"
        "v_mfma_f32_16x16x32_bf16 v[44:47], v[208:211], v[116:119], v[44:47]\n\t"
        "s_waitcnt vmcnt(0)\n\t"
        "s_mov_b64 exec, 1\n\t"
        "buffer_store_dwordx2 v[248:249], v250, s[76:79], s90 offen offset:1024 sc1\n\t"
        "s_mov_b64 exec, -1\n\t"
        "s_barrier\n\t"
        "v_mfma_f32_16x16x32_bf16 v[44:47], v[212:215], v[132:135], v[44:47]\n\t"
        "ds_read_b128 v[144:147], v232 offset:0\n\t"
        "v_mfma_f32_16x16x32_bf16 v[72:75], v[208:211], v[120:123], v[72:75]\n\t"
        "ds_read_b128 v[176:179], v234 offset:32768\n\t"
        "v_mfma_f32_16x16x32_bf16 v[72:75], v[212:215], v[136:139], v[72:75]\n\t"
        "ds_read_b128 v[160:163], v233 offset:0\n\t"
        "v_mfma_f32_16x16x32_bf16 v[100:103], v[208:211], v[124:127], v[100:103]\n\t"
        "ds_read_b128 v[180:183], v235 offset:32768\n\t"
        "v_mfma_f32_16x16x32_bf16 v[100:103], v[212:215], v[140:143], v[100:103]\n\t"
        "ds_read_b128 v[148:151], v232 offset:2048\n\t"
        "v_mfma_f32_16x16x32_bf16 v[104:107], v[216:219], v[124:127], v[104:107]\n\t"
        "ds_read_b128 v[164:167], v233 offset:2048\n\t"
        "v_mfma_f32_16x16x32_bf16 v[104:107], v[220:223], v[140:143], v[104:107]\n\t"
        "ds_read_b128 v[152:155], v232 offset:4096\n\t"
        "v_mfma_f32_16x16x32_bf16 v[76:79], v[216:219], v[120:123], v[76:79]\n\t"
        "ds_read_b128 v[168:171], v233 offset:4096\n\t"
        "v_mfma_f32_16x16x32_bf16 v[76:79], v[220:223], v[136:139], v[76:79]\n\t"
        "ds_read_b128 v[156:159], v232 offset:6144\n\t"
        "v_mfma_f32_16x16x32_bf16 v[48:51], v[216:219], v[116:119], v[48:51]\n\t"
        "ds_read_b128 v[172:175], v233 offset:6144\n\t"
        "v_mfma_f32_16x16x32_bf16 v[48:51], v[220:223], v[132:135], v[48:51]\n\t"
        "ds_read_b128 v[184:187], v234 offset:34816\n\t"
        "v_mfma_f32_16x16x32_bf16 v[20:23], v[216:219], v[112:115], v[20:23]\n\t"
        "ds_read_b128 v[188:191], v235 offset:34816\n\t"
        "v_mfma_f32_16x16x32_bf16 v[20:23], v[220:223], v[128:131], v[20:23]\n\t"
        "ds_read_b128 v[192:195], v234 offset:36864\n\t"
        "v_mfma_f32_16x16x32_bf16 v[24:27], v[224:227], v[112:115], v[24:27]\n\t"
        "ds_read_b128 v[196:199], v235 offset:36864\n\t"
        "v_mfma_f32_16x16x32_bf16 v[24:27], v[228:231], v[128:131], v[24:27]\n\t"
        "ds_read_b128 v[200:203], v234 offset:38912\n\t"
        "v_mfma_f32_16x16x32_bf16 v[52:55], v[224:227], v[116:119], v[52:55]\n\t"
        "ds_read_b128 v[204:207], v235 offset:38912\n\t"
        "v_mfma_f32_16x16x32_bf16 v[52:55], v[228:231], v[132:135], v[52:55]\n\t"
        "v_mfma_f32_16x16x32_bf16 v[80:83], v[224:227], v[120:123], v[80:83]\n\t"
        "v_mfma_f32_16x16x32_bf16 v[80:83], v[228:231], v[136:139], v[80:83]\n\t"
        "v_mfma_f32_16x16x32_bf16 v[108:111], v[224:227], v[124:127], v[108:111]\n\t"
        "v_mfma_f32_16x16x32_bf16 v[108:111], v[228:231], v[140:143], v[108:111]\n\t"
        "s_waitcnt lgkmcnt(0)\n\t"
        "v_mfma_f32_16x16x32_bf16 v[0:3], v[176:179], v[144:147], v[0:3]\n\t"
        "ds_read_b128 v[208:211], v234 offset:40960\n\t"
        "v_mfma_f32_16x16x32_bf16 v[0:3], v[180:183], v[160:163], v[0:3]\n\t"
        "ds_read_b128 v[212:215], v235 offset:40960\n\t"
        "v_mfma_f32_16x16x32_bf16 v[28:31], v[176:179], v[148:151], v[28:31]\n\t"
        "ds_read_b128 v[216:219], v234 offset:43008\n\t"
        "v_mfma_f32_16x16x32_bf16 v[28:31], v[180:183], v[164:167], v[28:31]\n\t"
        "ds_read_b128 v[220:223], v235 offset:43008\n\t"
        "v_mfma_f32_16x16x32_bf16 v[56:59], v[176:179], v[152:155], v[56:59]\n\t"
        "ds_read_b128 v[224:227], v234 offset:45056\n\t"
        "v_mfma_f32_16x16x32_bf16 v[56:59], v[180:183], v[168:171], v[56:59]\n\t"
        "ds_read_b128 v[228:231], v235 offset:45056\n\t"
        "v_mfma_f32_16x16x32_bf16 v[84:87], v[176:179], v[156:159], v[84:87]\n\t"
        "v_mfma_f32_16x16x32_bf16 v[84:87], v[180:183], v[172:175], v[84:87]\n\t"
        "v_mfma_f32_16x16x32_bf16 v[88:91], v[184:187], v[156:159], v[88:91]\n\t"
        "v_mfma_f32_16x16x32_bf16 v[88:91], v[188:191], v[172:175], v[88:91]\n\t"
        "v_mfma_f32_16x16x32_bf16 v[60:63], v[184:187], v[152:155], v[60:63]\n\t"
        "v_mfma_f32_16x16x32_bf16 v[60:63], v[188:191], v[168:171], v[60:63]\n\t"
        "v_mfma_f32_16x16x32_bf16 v[32:35], v[184:187], v[148:151], v[32:35]\n\t"
        "v_add_u32 v232, s48, v232\n\t"
        "v_mfma_f32_16x16x32_bf16 v[32:35], v[188:191], v[164:167], v[32:35]\n\t"
        "v_add_u32 v233, s48, v233\n\t"
        "v_mfma_f32_16x16x32_bf16 v[4:7], v[184:187], v[144:147], v[4:7]\n\t"
        "v_add_u32 v234, s48, v234\n\t"
        "v_mfma_f32_16x16x32_bf16 v[4:7], v[188:191], v[160:163], v[4:7]\n\t"
        "v_add_u32 v235, s48, v235\n\t"
        "v_mfma_f32_16x16x32_bf16 v[8:11], v[192:195], v[144:147], v[8:11]\n\t"
        "v_mfma_f32_16x16x32_bf16 v[8:11], v[196:199], v[160:163], v[8:11]\n\t"
        "v_mfma_f32_16x16x32_bf16 v[36:39], v[192:195], v[148:151], v[36:39]\n\t"
        "v_mfma_f32_16x16x32_bf16 v[36:39], v[196:199], v[164:167], v[36:39]\n\t"
        "v_mfma_f32_16x16x32_bf16 v[64:67], v[192:195], v[152:155], v[64:67]\n\t"
        "v_mfma_f32_16x16x32_bf16 v[64:67], v[196:199], v[168:171], v[64:67]\n\t"
        "v_mfma_f32_16x16x32_bf16 v[92:95], v[192:195], v[156:159], v[92:95]\n\t"
        "v_mfma_f32_16x16x32_bf16 v[92:95], v[196:199], v[172:175], v[92:95]\n\t"
        "v_mfma_f32_16x16x32_bf16 v[96:99], v[200:203], v[156:159], v[96:99]\n\t"
        "v_mfma_f32_16x16x32_bf16 v[96:99], v[204:207], v[172:175], v[96:99]\n\t"
        "v_mfma_f32_16x16x32_bf16 v[68:71], v[200:203], v[152:155], v[68:71]\n\t"
        "v_mfma_f32_16x16x32_bf16 v[68:71], v[204:207], v[168:171], v[68:71]\n\t"
        "v_mfma_f32_16x16x32_bf16 v[40:43], v[200:203], v[148:151], v[40:43]\n\t"
        "v_mfma_f32_16x16x32_bf16 v[40:43], v[204:207], v[164:167], v[40:43]\n\t"
        "v_mfma_f32_16x16x32_bf16 v[12:15], v[200:203], v[144:147], v[12:15]\n\t"
        "v_mfma_f32_16x16x32_bf16 v[12:15], v[204:207], v[160:163], v[12:15]\n\t"
        "s_waitcnt lgkmcnt(0)\n\t"
        "v_mfma_f32_16x16x32_bf16 v[16:19], v[208:211], v[144:147], v[16:19]\n\t"
        "v_mfma_f32_16x16x32_bf16 v[16:19], v[212:215], v[160:163], v[16:19]\n\t"
        "v_mfma_f32_16x16x32_bf16 v[44:47], v[208:211], v[148:151], v[44:47]\n\t"
        "s_barrier\n\t"
        "v_mfma_f32_16x16x32_bf16 v[44:47], v[212:215], v[164:167], v[44:47]\n\t"
        "ds_read_b128 v[112:115], v232 offset:0\n\t"
        "v_mfma_f32_16x16x32_bf16 v[72:75], v[208:211], v[152:155], v[72:75]\n\t"
        "ds_read_b128 v[176:179], v234 offset:32768\n\t"
        "v_mfma_f32_16x16x32_bf16 v[72:75], v[212:215], v[168:171], v[72:75]\n\t"
        "ds_read_b128 v[128:131], v233 offset:0\n\t"
        "v_mfma_f32_16x16x32_bf16 v[100:103], v[208:211], v[156:159], v[100:103]\n\t"
        "ds_read_b128 v[180:183], v235 offset:32768\n\t"
        "v_mfma_f32_16x16x32_bf16 v[100:103], v[212:215], v[172:175], v[100:103]\n\t"
        "ds_read_b128 v[116:119], v232 offset:2048\n\t"
        "v_mfma_f32_16x16x32_bf16 v[104:107], v[216:219], v[156:159], v[104:107]\n\t"
        "ds_read_b128 v[132:135], v233 offset:2048\n\t"
        "v_mfma_f32_16x16x32_bf16 v[104:107], v[220:223], v[172:175], v[104:107]\n\t"
        "ds_read_b128 v[120:123], v232 offset:4096\n\t"
        "v_mfma_f32_16x16x32_bf16 v[76:79], v[216:219], v[152:155], v[76:79]\n\t"
        "ds_read_b128 v[136:139], v233 offset:4096\n\t"
        "v_mfma_f32_16x16x32_bf16 v[76:79], v[220:223], v[168:171], v[76:79]\n\t"
        "ds_read_b128 v[124:127], v232 offset:6144\n\t"
        "v_mfma_f32_16x16x32_bf16 v[48:51], v[216:219], v[148:151], v[48:51]\n\t"
        "ds_read_b128 v[140:143], v233 offset:6144\n\t"
        "v_mfma_f32_16x16x32_bf16 v[48:51], v[220:223], v[164:167], v[48:51]\n\t"
        "ds_read_b128 v[184:187], v234 offset:34816\n\t"
        "v_mfma_f32_16x16x32_bf16 v[20:23], v[216:219], v[144:147], v[20:23]\n\t"
        "ds_read_b128 v[188:191], v235 offset:34816\n\t"
        "v_mfma_f32_16x16x32_bf16 v[20:23], v[220:223], v[160:163], v[20:23]\n\t"
        "ds_read_b128 v[192:195], v234 offset:36864\n\t"
        "v_mfma_f32_16x16x32_bf16 v[24:27], v[224:227], v[144:147], v[24:27]\n\t"
        "ds_read_b128 v[196:199], v235 offset:36864\n\t"
        "v_mfma_f32_16x16x32_bf16 v[24:27], v[228:231], v[160:163], v[24:27]\n\t"
        "ds_read_b128 v[200:203], v234 offset:38912\n\t"
        "v_mfma_f32_16x16x32_bf16 v[52:55], v[224:227], v[148:151], v[52:55]\n\t"
        "ds_read_b128 v[204:207], v235 offset:38912\n\t"
        "v_mfma_f32_16x16x32_bf16 v[52:55], v[228:231], v[164:167], v[52:55]\n\t"
        "v_mfma_f32_16x16x32_bf16 v[80:83], v[224:227], v[152:155], v[80:83]\n\t"
        "v_mfma_f32_16x16x32_bf16 v[80:83], v[228:231], v[168:171], v[80:83]\n\t"
        "v_mfma_f32_16x16x32_bf16 v[108:111], v[224:227], v[156:159], v[108:111]\n\t"
        "v_mfma_f32_16x16x32_bf16 v[108:111], v[228:231], v[172:175], v[108:111]\n\t"
        "s_mov_b32 s40, 1\n\t"
        "s_mov_b32 s41, 12\n\t"
        "L_cloop_%=:\n\t"
        "s_waitcnt lgkmcnt(0)\n\t"
        "v_mfma_f32_16x16x32_bf16 v[0:3], v[176:179], v[112:115], v[0:3]\n\t"
        "ds_read_b128 v[208:211], v234 offset:40960\n\t"
        "v_mfma_f32_16x16x32_bf16 v[0:3], v[180:183], v[128:131], v[0:3]\n\t"
        "ds_read_b128 v[212:215], v235 offset:40960\n\t"
        "v_mfma_f32_16x16x32_bf16 v[28:31], v[176:179], v[116:119], v[28:31]\n\t"
        "ds_read_b128 v[216:219], v234 offset:43008\n\t"
        "s_cmp_eq_u32 s40, 2\n\t"
        "v_mfma_f32_16x16x32_bf16 v[28:31], v[180:183], v[132:135], v[28:31]\n\t"
        "ds_read_b128 v[220:223], v235 offset:43008\n\t"
        "s_cselect_b32 s42, s47, s48\n\t"
        "v_mfma_f32_16x16x32_bf16 v[56:59], v[176:179], v[120:123], v[56:59]\n\t"
        "ds_read_b128 v[224:227], v234 offset:45056\n\t"
        "s_add_u32 s40, s40, 1\n\t"
        "v_mfma_f32_16x16x32_bf16 v[56:59], v[180:183], v[136:139], v[56:59]\n\t"
        "ds_read_b128 v[228:231], v235 offset:45056\n\t"
        "s_cmp_eq_u32 s40, 3\n\t"
        "v_mfma_f32_16x16x32_bf16 v[84:87], v[176:179], v[124:127], v[84:87]\n\t"
        "s_cselect_b32 s40, 0, s40\n\t"
        "v_mfma_f32_16x16x32_bf16 v[84:87], v[180:183], v[140:143], v[84:87]\n\t"
        "v_mfma_f32_16x16x32_bf16 v[88:91], v[184:187], v[124:127], v[88:91]\n\t"
        "v_mfma_f32_16x16x32_bf16 v[88:91], v[188:191], v[140:143], v[88:91]\n\t"
        "v_mfma_f32_16x16x32_bf16 v[60:63], v[184:187], v[120:123], v[60:63]\n\t"
        "v_mfma_f32_16x16x32_bf16 v[60:63], v[188:191], v[136:139], v[60:63]\n\t"
        "v_mfma_f32_16x16x32_bf16 v[32:35], v[184:187], v[116:119], v[32:35]\n\t"
        "v_add_u32 v232, s42, v232\n\t"
        "v_mfma_f32_16x16x32_bf16 v[32:35], v[188:191], v[132:135], v[32:35]\n\t"
        "v_add_u32 v233, s42, v233\n\t"
        "v_mfma_f32_16x16x32_bf16 v[4:7], v[184:187], v[112:115], v[4:7]\n\t"
        "v_add_u32 v234, s42, v234\n\t"
        "v_mfma_f32_16x16x32_bf16 v[4:7], v[188:191], v[128:131], v[4:7]\n\t"
        "v_add_u32 v235, s42, v235\n\t"
        "v_mfma_f32_16x16x32_bf16 v[8:11], v[192:195], v[112:115], v[8:11]\n\t"
        "v_mfma_f32_16x16x32_bf16 v[8:11], v[196:199], v[128:131], v[8:11]\n\t"
        "v_mfma_f32_16x16x32_bf16 v[36:39], v[192:195], v[116:119], v[36:39]\n\t"
        "v_mfma_f32_16x16x32_bf16 v[36:39], v[196:199], v[132:135], v[36:39]\n\t"
        "v_mfma_f32_16x16x32_bf16 v[64:67], v[192:195], v[120:123], v[64:67]\n\t"
        "v_mfma_f32_16x16x32_bf16 v[64:67], v[196:199], v[136:139], v[64:67]\n\t"
        "v_mfma_f32_16x16x32_bf16 v[92:95], v[192:195], v[124:127], v[92:95]\n\t"
        "v_mfma_f32_16x16x32_bf16 v[92:95], v[196:199], v[140:143], v[92:95]\n\t"
        "v_mfma_f32_16x16x32_bf16 v[96:99], v[200:203], v[124:127], v[96:99]\n\t"
        "v_mfma_f32_16x16x32_bf16 v[96:99], v[204:207], v[140:143], v[96:99]\n\t"
        "v_mfma_f32_16x16x32_bf16 v[68:71], v[200:203], v[120:123], v[68:71]\n\t"
        "v_mfma_f32_16x16x32_bf16 v[68:71], v[204:207], v[136:139], v[68:71]\n\t"
        "v_mfma_f32_16x16x32_bf16 v[40:43], v[200:203], v[116:119], v[40:43]\n\t"
        "v_mfma_f32_16x16x32_bf16 v[40:43], v[204:207], v[132:135], v[40:43]\n\t"
        "v_mfma_f32_16x16x32_bf16 v[12:15], v[200:203], v[112:115], v[12:15]\n\t"
        "v_mfma_f32_16x16x32_bf16 v[12:15], v[204:207], v[128:131], v[12:15]\n\t"
        "s_waitcnt lgkmcnt(0)\n\t"
        "v_mfma_f32_16x16x32_bf16 v[16:19], v[208:211], v[112:115], v[16:19]\n\t"
        "v_mfma_f32_16x16x32_bf16 v[16:19], v[212:215], v[128:131], v[16:19]\n\t"
        "v_mfma_f32_16x16x32_bf16 v[44:47], v[208:211], v[116:119], v[44:47]\n\t"
        "s_barrier\n\t"
        "v_mfma_f32_16x16x32_bf16 v[44:47], v[212:215], v[132:135], v[44:47]\n\t"
        "ds_read_b128 v[144:147], v232 offset:0\n\t"
        "v_mfma_f32_16x16x32_bf16 v[72:75], v[208:211], v[120:123], v[72:75]\n\t"
        "ds_read_b128 v[176:179], v234 offset:32768\n\t"
        "v_mfma_f32_16x16x32_bf16 v[72:75], v[212:215], v[136:139], v[72:75]\n\t"
        "ds_read_b128 v[160:163], v233 offset:0\n\t"
        "v_mfma_f32_16x16x32_bf16 v[100:103], v[208:211], v[124:127], v[100:103]\n\t"
        "ds_read_b128 v[180:183], v235 offset:32768\n\t"
        "v_mfma_f32_16x16x32_bf16 v[100:103], v[212:215], v[140:143], v[100:103]\n\t"
        "ds_read_b128 v[148:151], v232 offset:2048\n\t"
        "v_mfma_f32_16x16x32_bf16 v[104:107], v[216:219], v[124:127], v[104:107]\n\t"
        "ds_read_b128 v[164:167], v233 offset:2048\n\t"
        "v_mfma_f32_16x16x32_bf16 v[104:107], v[220:223], v[140:143], v[104:107]\n\t"
        "ds_read_b128 v[152:155], v232 offset:4096\n\t"
        "v_mfma_f32_16x16x32_bf16 v[76:79], v[216:219], v[120:123], v[76:79]\n\t"
        "ds_read_b128 v[168:171], v233 offset:4096\n\t"
        "v_mfma_f32_16x16x32_bf16 v[76:79], v[220:223], v[136:139], v[76:79]\n\t"
        "ds_read_b128 v[156:159], v232 offset:6144\n\t"
        "v_mfma_f32_16x16x32_bf16 v[48:51], v[216:219], v[116:119], v[48:51]\n\t"
        "ds_read_b128 v[172:175], v233 offset:6144\n\t"
        "v_mfma_f32_16x16x32_bf16 v[48:51], v[220:223], v[132:135], v[48:51]\n\t"
        "ds_read_b128 v[184:187], v234 offset:34816\n\t"
        "v_mfma_f32_16x16x32_bf16 v[20:23], v[216:219], v[112:115], v[20:23]\n\t"
        "ds_read_b128 v[188:191], v235 offset:34816\n\t"
        "v_mfma_f32_16x16x32_bf16 v[20:23], v[220:223], v[128:131], v[20:23]\n\t"
        "ds_read_b128 v[192:195], v234 offset:36864\n\t"
        "v_mfma_f32_16x16x32_bf16 v[24:27], v[224:227], v[112:115], v[24:27]\n\t"
        "ds_read_b128 v[196:199], v235 offset:36864\n\t"
        "v_mfma_f32_16x16x32_bf16 v[24:27], v[228:231], v[128:131], v[24:27]\n\t"
        "ds_read_b128 v[200:203], v234 offset:38912\n\t"
        "v_mfma_f32_16x16x32_bf16 v[52:55], v[224:227], v[116:119], v[52:55]\n\t"
        "ds_read_b128 v[204:207], v235 offset:38912\n\t"
        "v_mfma_f32_16x16x32_bf16 v[52:55], v[228:231], v[132:135], v[52:55]\n\t"
        "v_mfma_f32_16x16x32_bf16 v[80:83], v[224:227], v[120:123], v[80:83]\n\t"
        "v_mfma_f32_16x16x32_bf16 v[80:83], v[228:231], v[136:139], v[80:83]\n\t"
        "s_sub_u32 s41, s41, 1\n\t"
        "v_mfma_f32_16x16x32_bf16 v[108:111], v[224:227], v[124:127], v[108:111]\n\t"
        "v_mfma_f32_16x16x32_bf16 v[108:111], v[228:231], v[140:143], v[108:111]\n\t"
        "s_waitcnt lgkmcnt(0)\n\t"
        "v_mfma_f32_16x16x32_bf16 v[0:3], v[176:179], v[144:147], v[0:3]\n\t"
        "ds_read_b128 v[208:211], v234 offset:40960\n\t"
        "v_mfma_f32_16x16x32_bf16 v[0:3], v[180:183], v[160:163], v[0:3]\n\t"
        "ds_read_b128 v[212:215], v235 offset:40960\n\t"
        "v_mfma_f32_16x16x32_bf16 v[28:31], v[176:179], v[148:151], v[28:31]\n\t"
        "ds_read_b128 v[216:219], v234 offset:43008\n\t"
        "s_cmp_eq_u32 s40, 2\n\t"
        "v_mfma_f32_16x16x32_bf16 v[28:31], v[180:183], v[164:167], v[28:31]\n\t"
        "ds_read_b128 v[220:223], v235 offset:43008\n\t"
        "s_cselect_b32 s42, s47, s48\n\t"
        "v_mfma_f32_16x16x32_bf16 v[56:59], v[176:179], v[152:155], v[56:59]\n\t"
        "ds_read_b128 v[224:227], v234 offset:45056\n\t"
        "s_add_u32 s40, s40, 1\n\t"
        "v_mfma_f32_16x16x32_bf16 v[56:59], v[180:183], v[168:171], v[56:59]\n\t"
        "ds_read_b128 v[228:231], v235 offset:45056\n\t"
        "s_cmp_eq_u32 s40, 3\n\t"
        "v_mfma_f32_16x16x32_bf16 v[84:87], v[176:179], v[156:159], v[84:87]\n\t"
        "s_cselect_b32 s40, 0, s40\n\t"
        "v_mfma_f32_16x16x32_bf16 v[84:87], v[180:183], v[172:175], v[84:87]\n\t"
        "v_mfma_f32_16x16x32_bf16 v[88:91], v[184:187], v[156:159], v[88:91]\n\t"
        "v_mfma_f32_16x16x32_bf16 v[88:91], v[188:191], v[172:175], v[88:91]\n\t"
        "v_mfma_f32_16x16x32_bf16 v[60:63], v[184:187], v[152:155], v[60:63]\n\t"
        "v_mfma_f32_16x16x32_bf16 v[60:63], v[188:191], v[168:171], v[60:63]\n\t"
        "v_mfma_f32_16x16x32_bf16 v[32:35], v[184:187], v[148:151], v[32:35]\n\t"
        "v_add_u32 v232, s42, v232\n\t"
        "v_mfma_f32_16x16x32_bf16 v[32:35], v[188:191], v[164:167], v[32:35]\n\t"
        "v_add_u32 v233, s42, v233\n\t"
        "v_mfma_f32_16x16x32_bf16 v[4:7], v[184:187], v[144:147], v[4:7]\n\t"
        "v_add_u32 v234, s42, v234\n\t"
        "v_mfma_f32_16x16x32_bf16 v[4:7], v[188:191], v[160:163], v[4:7]\n\t"
        "v_add_u32 v235, s42, v235\n\t"
        "v_mfma_f32_16x16x32_bf16 v[8:11], v[192:195], v[144:147], v[8:11]\n\t"
        "v_mfma_f32_16x16x32_bf16 v[8:11], v[196:199], v[160:163], v[8:11]\n\t"
        "v_mfma_f32_16x16x32_bf16 v[36:39], v[192:195], v[148:151], v[36:39]\n\t"
        "v_mfma_f32_16x16x32_bf16 v[36:39], v[196:199], v[164:167], v[36:39]\n\t"
        "v_mfma_f32_16x16x32_bf16 v[64:67], v[192:195], v[152:155], v[64:67]\n\t"
        "v_mfma_f32_16x16x32_bf16 v[64:67], v[196:199], v[168:171], v[64:67]\n\t"
        "v_mfma_f32_16x16x32_bf16 v[92:95], v[192:195], v[156:159], v[92:95]\n\t"
        "v_mfma_f32_16x16x32_bf16 v[92:95], v[196:199], v[172:175], v[92:95]\n\t"
        "v_mfma_f32_16x16x32_bf16 v[96:99], v[200:203], v[156:159], v[96:99]\n\t"
        "v_mfma_f32_16x16x32_bf16 v[96:99], v[204:207], v[172:175], v[96:99]\n\t"
        "v_mfma_f32_16x16x32_bf16 v[68:71], v[200:203], v[152:155], v[68:71]\n\t"
        "v_mfma_f32_16x16x32_bf16 v[68:71], v[204:207], v[168:171], v[68:71]\n\t"
        "v_mfma_f32_16x16x32_bf16 v[40:43], v[200:203], v[148:151], v[40:43]\n\t"
        "v_mfma_f32_16x16x32_bf16 v[40:43], v[204:207], v[164:167], v[40:43]\n\t"
        "v_mfma_f32_16x16x32_bf16 v[12:15], v[200:203], v[144:147], v[12:15]\n\t"
        "v_mfma_f32_16x16x32_bf16 v[12:15], v[204:207], v[160:163], v[12:15]\n\t"
        "s_waitcnt lgkmcnt(0)\n\t"
        "v_mfma_f32_16x16x32_bf16 v[16:19], v[208:211], v[144:147], v[16:19]\n\t"
        "v_mfma_f32_16x16x32_bf16 v[16:19], v[212:215], v[160:163], v[16:19]\n\t"
        "v_mfma_f32_16x16x32_bf16 v[44:47], v[208:211], v[148:151], v[44:47]\n\t"
        "s_barrier\n\t"
        "v_mfma_f32_16x16x32_bf16 v[44:47], v[212:215], v[164:167], v[44:47]\n\t"
        "ds_read_b128 v[112:115], v232 offset:0\n\t"
        "v_mfma_f32_16x16x32_bf16 v[72:75], v[208:211], v[152:155], v[72:75]\n\t"
        "ds_read_b128 v[176:179], v234 offset:32768\n\t"
        "v_mfma_f32_16x16x32_bf16 v[72:75], v[212:215], v[168:171], v[72:75]\n\t"
        "ds_read_b128 v[128:131], v233 offset:0\n\t"
        "v_mfma_f32_16x16x32_bf16 v[100:103], v[208:211], v[156:159], v[100:103]\n\t"
        "ds_read_b128 v[180:183], v235 offset:32768\n\t"
        "v_mfma_f32_16x16x32_bf16 v[100:103], v[212:215], v[172:175], v[100:103]\n\t"
        "ds_read_b128 v[116:119], v232 offset:2048\n\t"
        "v_mfma_f32_16x16x32_bf16 v[104:107], v[216:219], v[156:159], v[104:107]\n\t"
        "ds_read_b128 v[132:135], v233 offset:2048\n\t"
        "v_mfma_f32_16x16x32_bf16 v[104:107], v[220:223], v[172:175], v[104:107]\n\t"
        "ds_read_b128 v[120:123], v232 offset:4096\n\t"
        "v_mfma_f32_16x16x32_bf16 v[76:79], v[216:219], v[152:155], v[76:79]\n\t"
        "ds_read_b128 v[136:139], v233 offset:4096\n\t"
        "v_mfma_f32_16x16x32_bf16 v[76:79], v[220:223], v[168:171], v[76:79]\n\t"
        "ds_read_b128 v[124:127], v232 offset:6144\n\t"
        "v_mfma_f32_16x16x32_bf16 v[48:51], v[216:219], v[148:151], v[48:51]\n\t"
        "ds_read_b128 v[140:143], v233 offset:6144\n\t"
        "v_mfma_f32_16x16x32_bf16 v[48:51], v[220:223], v[164:167], v[48:51]\n\t"
        "ds_read_b128 v[184:187], v234 offset:34816\n\t"
        "v_mfma_f32_16x16x32_bf16 v[20:23], v[216:219], v[144:147], v[20:23]\n\t"
        "ds_read_b128 v[188:191], v235 offset:34816\n\t"
        "v_mfma_f32_16x16x32_bf16 v[20:23], v[220:223], v[160:163], v[20:23]\n\t"
        "ds_read_b128 v[192:195], v234 offset:36864\n\t"
        "v_mfma_f32_16x16x32_bf16 v[24:27], v[224:227], v[144:147], v[24:27]\n\t"
        "ds_read_b128 v[196:199], v235 offset:36864\n\t"
        "v_mfma_f32_16x16x32_bf16 v[24:27], v[228:231], v[160:163], v[24:27]\n\t"
        "ds_read_b128 v[200:203], v234 offset:38912\n\t"
        "v_mfma_f32_16x16x32_bf16 v[52:55], v[224:227], v[148:151], v[52:55]\n\t"
        "ds_read_b128 v[204:207], v235 offset:38912\n\t"
        "v_mfma_f32_16x16x32_bf16 v[52:55], v[228:231], v[164:167], v[52:55]\n\t"
        "v_mfma_f32_16x16x32_bf16 v[80:83], v[224:227], v[152:155], v[80:83]\n\t"
        "v_mfma_f32_16x16x32_bf16 v[80:83], v[228:231], v[168:171], v[80:83]\n\t"
        "s_sub_u32 s41, s41, 1\n\t"
        "v_mfma_f32_16x16x32_bf16 v[108:111], v[224:227], v[156:159], v[108:111]\n\t"
        "s_cmp_lg_u32 s41, 2\n\t"
        "v_mfma_f32_16x16x32_bf16 v[108:111], v[228:231], v[172:175], v[108:111]\n\t"
        "s_cbranch_scc1 L_cloop_%=\n\t"
        "v_add_u32 v242, s47, v232\n\t"
        "v_add_u32 v248, s47, v234\n\t"
        "v_add_u32 v243, s47, v233\n\t"
        "v_add_u32 v249, s47, v235\n\t"
        "s_waitcnt lgkmcnt(0)\n\t"
        "v_mfma_f32_16x16x32_bf16 v[0:3], v[176:179], v[112:115], v[0:3]\n\t"
        "ds_read_b128 v[208:211], v234 offset:40960\n\t"
        "v_mfma_f32_16x16x32_bf16 v[0:3], v[180:183], v[128:131], v[0:3]\n\t"
        "ds_read_b128 v[212:215], v235 offset:40960\n\t"
        "v_mfma_f32_16x16x32_bf16 v[28:31], v[176:179], v[116:119], v[28:31]\n\t"
        "ds_read_b128 v[216:219], v234 offset:43008\n\t"
        "v_mfma_f32_16x16x32_bf16 v[28:31], v[180:183], v[132:135], v[28:31]\n\t"
        "ds_read_b128 v[220:223], v235 offset:43008\n\t"
        "v_mfma_f32_16x16x32_bf16 v[32:35], v[184:187], v[116:119], v[32:35]\n\t"
        "ds_read_b128 v[224:227], v234 offset:45056\n\t"
        "v_mfma_f32_16x16x32_bf16 v[32:35], v[188:191], v[132:135], v[32:35]\n\t"
        "ds_read_b128 v[228:231], v235 offset:45056\n\t"
        "v_mfma_f32_16x16x32_bf16 v[4:7], v[184:187], v[112:115], v[4:7]\n\t"
        "v_mfma_f32_16x16x32_bf16 v[4:7], v[188:191], v[128:131], v[4:7]\n\t"
        "v_mfma_f32_16x16x32_bf16 v[8:11], v[192:195], v[112:115], v[8:11]\n\t"
        "v_mfma_f32_16x16x32_bf16 v[8:11], v[196:199], v[128:131], v[8:11]\n\t"
        "v_mfma_f32_16x16x32_bf16 v[36:39], v[192:195], v[116:119], v[36:39]\n\t"
        "v_mfma_f32_16x16x32_bf16 v[36:39], v[196:199], v[132:135], v[36:39]\n\t"
        "v_mfma_f32_16x16x32_bf16 v[40:43], v[200:203], v[116:119], v[40:43]\n\t"
        "v_mfma_f32_16x16x32_bf16 v[40:43], v[204:207], v[132:135], v[40:43]\n\t"
        "v_mfma_f32_16x16x32_bf16 v[12:15], v[200:203], v[112:115], v[12:15]\n\t"
        "v_mfma_f32_16x16x32_bf16 v[12:15], v[204:207], v[128:131], v[12:15]\n\t"
        "s_waitcnt lgkmcnt(0)\n\t"
        "s_barrier\n\t"
        "v_mfma_f32_16x16x32_bf16 v[16:19], v[208:211], v[112:115], v[16:19]\n\t"
        "ds_read_b128 v[144:147], v242 offset:0\n\t"
        "v_mfma_f32_16x16x32_bf16 v[16:19], v[212:215], v[128:131], v[16:19]\n\t"
        "ds_read_b128 v[176:179], v248 offset:32768\n\t"
        "v_mfma_f32_16x16x32_bf16 v[44:47], v[208:211], v[116:119], v[44:47]\n\t"
        "ds_read_b128 v[160:163], v243 offset:0\n\t"
        "v_mfma_f32_16x16x32_bf16 v[44:47], v[212:215], v[132:135], v[44:47]\n\t"
        "ds_read_b128 v[180:183], v249 offset:32768\n\t"
        "v_mfma_f32_16x16x32_bf16 v[48:51], v[216:219], v[116:119], v[48:51]\n\t"
        "ds_read_b128 v[148:151], v242 offset:2048\n\t"
        "v_mfma_f32_16x16x32_bf16 v[48:51], v[220:223], v[132:135], v[48:51]\n\t"
        "ds_read_b128 v[164:167], v243 offset:2048\n\t"
        "v_mfma_f32_16x16x32_bf16 v[20:23], v[216:219], v[112:115], v[20:23]\n\t"
        "ds_read_b128 v[184:187], v248 offset:34816\n\t"
        "v_mfma_f32_16x16x32_bf16 v[20:23], v[220:223], v[128:131], v[20:23]\n\t"
        "ds_read_b128 v[188:191], v249 offset:34816\n\t"
        "v_mfma_f32_16x16x32_bf16 v[24:27], v[224:227], v[112:115], v[24:27]\n\t"
        "ds_read_b128 v[192:195], v248 offset:36864\n\t"
        "v_mfma_f32_16x16x32_bf16 v[24:27], v[228:231], v[128:131], v[24:27]\n\t"
        "ds_read_b128 v[196:199], v249 offset:36864\n\t"
        "v_mfma_f32_16x16x32_bf16 v[52:55], v[224:227], v[116:119], v[52:55]\n\t"
        "ds_read_b128 v[200:203], v248 offset:38912\n\t"
        "v_mfma_f32_16x16x32_bf16 v[52:55], v[228:231], v[132:135], v[52:55]\n\t"
        "ds_read_b128 v[204:207], v249 offset:38912\n\t"
        "s_waitcnt lgkmcnt(0)\n\t"
        "v_mfma_f32_16x16x32_bf16 v[0:3], v[176:179], v[144:147], v[0:3]\n\t"
        "ds_read_b128 v[208:211], v248 offset:40960\n\t"
        "v_mfma_f32_16x16x32_bf16 v[0:3], v[180:183], v[160:163], v[0:3]\n\t"
        "ds_read_b128 v[212:215], v249 offset:40960\n\t"
        "v_mfma_f32_16x16x32_bf16 v[28:31], v[176:179], v[148:151], v[28:31]\n\t"
        "ds_read_b128 v[216:219], v248 offset:43008\n\t"
        "v_mfma_f32_16x16x32_bf16 v[28:31], v[180:183], v[164:167], v[28:31]\n\t"
        "ds_read_b128 v[220:223], v249 offset:43008\n\t"
        "v_mfma_f32_16x16x32_bf16 v[32:35], v[184:187], v[148:151], v[32:35]\n\t"
        "ds_read_b128 v[224:227], v248 offset:45056\n\t"
        "v_mfma_f32_16x16x32_bf16 v[32:35], v[188:191], v[164:167], v[32:35]\n\t"
        "ds_read_b128 v[228:231], v249 offset:45056\n\t"
        "v_mfma_f32_16x16x32_bf16 v[4:7], v[184:187], v[144:147], v[4:7]\n\t"
        "ds_read_b128 v[152:155], v242 offset:4096\n\t"
        "v_mfma_f32_16x16x32_bf16 v[4:7], v[188:191], v[160:163], v[4:7]\n\t"
        "ds_read_b128 v[168:171], v243 offset:4096\n\t"
        "v_mfma_f32_16x16x32_bf16 v[8:11], v[192:195], v[144:147], v[8:11]\n\t"
        "ds_read_b128 v[156:159], v242 offset:6144\n\t"
        "v_mfma_f32_16x16x32_bf16 v[8:11], v[196:199], v[160:163], v[8:11]\n\t"
        "ds_read_b128 v[172:175], v243 offset:6144\n\t"
        "v_mfma_f32_16x16x32_bf16 v[36:39], v[192:195], v[148:151], v[36:39]\n\t"
        "v_mfma_f32_16x16x32_bf16 v[36:39], v[196:199], v[164:167], v[36:39]\n\t"
        "v_mfma_f32_16x16x32_bf16 v[40:43], v[200:203], v[148:151], v[40:43]\n\t"
        "v_mfma_f32_16x16x32_bf16 v[40:43], v[204:207], v[164:167], v[40:43]\n\t"
        "v_mfma_f32_16x16x32_bf16 v[12:15], v[200:203], v[144:147], v[12:15]\n\t"
        "v_mfma_f32_16x16x32_bf16 v[12:15], v[204:207], v[160:163], v[12:15]\n\t"
        "s_waitcnt lgkmcnt(0)\n\t"
        "v_mfma_f32_16x16x32_bf16 v[16:19], v[208:211], v[144:147], v[16:19]\n\t"
        "ds_read_b128 v[176:179], v234 offset:32768\n\t"
        "v_mfma_f32_16x16x32_bf16 v[16:19], v[212:215], v[160:163], v[16:19]\n\t"
        "ds_read_b128 v[180:183], v235 offset:32768\n\t"
        "v_mfma_f32_16x16x32_bf16 v[44:47], v[208:211], v[148:151], v[44:47]\n\t"
        "ds_read_b128 v[184:187], v234 offset:34816\n\t"
        "v_cvt_pk_bf16_f32 v112, v0, v1\n\t"
        "v_cvt_pk_bf16_f32 v113, v2, v3\n\t"
        "v_mfma_f32_16x16x32_bf16 v[44:47], v[212:215], v[164:167], v[44:47]\n\t"
        "ds_read_b128 v[188:191], v235 offset:34816\n\t"
        "v_cvt_pk_bf16_f32 v114, v4, v5\n\t"
        "v_cvt_pk_bf16_f32 v115, v6, v7\n\t"
        "v_mfma_f32_16x16x32_bf16 v[48:51], v[216:219], v[148:151], v[48:51]\n\t"
        "ds_read_b128 v[192:195], v234 offset:36864\n\t"
        "ds_write_b128 v236, v[112:115] offset:0\n\t"
        "v_cvt_pk_bf16_f32 v116, v8, v9\n\t"
        "v_mfma_f32_16x16x32_bf16 v[48:51], v[220:223], v[164:167], v[48:51]\n\t"
        "ds_read_b128 v[196:199], v235 offset:36864\n\t"
        "v_cvt_pk_bf16_f32 v117, v10, v11\n\t"
        "v_cvt_pk_bf16_f32 v118, v12, v13\n\t"
        "v_mfma_f32_16x16x32_bf16 v[20:23], v[216:219], v[144:147], v[20:23]\n\t"
        "ds_read_b128 v[200:203], v234 offset:38912\n\t"
        "v_cvt_pk_bf16_f32 v119, v14, v15\n\t"
        "ds_write_b128 v236, v[116:119] offset:64\n\t"
        "v_mfma_f32_16x16x32_bf16 v[20:23], v[220:223], v[160:163], v[20:23]\n\t"
        "ds_read_b128 v[204:207], v235 offset:38912\n\t"
        "v_cvt_pk_bf16_f32 v112, v28, v29\n\t"
        "v_cvt_pk_bf16_f32 v113, v30, v31\n\t"
        "v_mfma_f32_16x16x32_bf16 v[24:27], v[224:227], v[144:147], v[24:27]\n\t"
        "v_cvt_pk_bf16_f32 v114, v32, v33\n\t"
        "v_cvt_pk_bf16_f32 v115, v34, v35\n\t"
        "v_mfma_f32_16x16x32_bf16 v[24:27], v[228:231], v[160:163], v[24:27]\n\t"
        "ds_write_b128 v236, v[112:115] offset:3840\n\t"
        "v_cvt_pk_bf16_f32 v116, v36, v37\n\t"
        "v_mfma_f32_16x16x32_bf16 v[52:55], v[224:227], v[148:151], v[52:55]\n\t"
        "v_cvt_pk_bf16_f32 v117, v38, v39\n\t"
        "v_cvt_pk_bf16_f32 v118, v40, v41\n\t"
        "v_mfma_f32_16x16x32_bf16 v[52:55], v[228:231], v[164:167], v[52:55]\n\t"
        "v_cvt_pk_bf16_f32 v119, v42, v43\n\t"
        "ds_write_b128 v236, v[116:119] offset:3904\n\t"
        "s_waitcnt lgkmcnt(0)\n\t"
        "v_mfma_f32_16x16x32_bf16 v[56:59], v[176:179], v[120:123], v[56:59]\n\t"
        "ds_read_b128 v[208:211], v234 offset:40960\n\t"
        "v_mfma_f32_16x16x32_bf16 v[56:59], v[180:183], v[136:139], v[56:59]\n\t"
        "ds_read_b128 v[212:215], v235 offset:40960\n\t"
        "v_mfma_f32_16x16x32_bf16 v[84:87], v[176:179], v[124:127], v[84:87]\n\t"
        "ds_read_b128 v[216:219], v234 offset:43008\n\t"
        "v_mfma_f32_16x16x32_bf16 v[84:87], v[180:183], v[140:143], v[84:87]\n\t"
        "ds_read_b128 v[220:223], v235 offset:43008\n\t"
        "v_mfma_f32_16x16x32_bf16 v[88:91], v[184:187], v[124:127], v[88:91]\n\t"
        "ds_read_b128 v[224:227], v234 offset:45056\n\t"
        "v_cvt_pk_bf16_f32 v128, v16, v17\n\t"
        "v_cvt_pk_bf16_f32 v129, v18, v19\n\t"
        "v_mfma_f32_16x16x32_bf16 v[88:91], v[188:191], v[140:143], v[88:91]\n\t"
        "ds_read_b128 v[228:231], v235 offset:45056\n\t"
        "v_cvt_pk_bf16_f32 v130, v20, v21\n\t"
        "v_cvt_pk_bf16_f32 v131, v22, v23\n\t"
        "v_mfma_f32_16x16x32_bf16 v[60:63], v[184:187], v[120:123], v[60:63]\n\t"
        "ds_write_b128 v236, v[128:131] offset:128\n\t"
        "v_cvt_pk_bf16_f32 v132, v24, v25\n\t"
        "v_mfma_f32_16x16x32_bf16 v[60:63], v[188:191], v[136:139], v[60:63]\n\t"
        "v_cvt_pk_bf16_f32 v133, v26, v27\n\t"
        "ds_write_b64 v237, v[132:133] offset:192\n\t"
        "v_mfma_f32_16x16x32_bf16 v[64:67], v[192:195], v[120:123], v[64:67]\n\t"
        "v_cvt_pk_bf16_f32 v128, v44, v45\n\t"
        "v_cvt_pk_bf16_f32 v129, v46, v47\n\t"
        "v_mfma_f32_16x16x32_bf16 v[64:67], v[196:199], v[136:139], v[64:67]\n\t"
        "v_cvt_pk_bf16_f32 v130, v48, v49\n\t"
        "v_cvt_pk_bf16_f32 v131, v50, v51\n\t"
        "v_mfma_f32_16x16x32_bf16 v[92:95], v[192:195], v[124:127], v[92:95]\n\t"
        "ds_write_b128 v236, v[128:131] offset:3968\n\t"
        "v_cvt_pk_bf16_f32 v132, v52, v53\n\t"
        "v_mfma_f32_16x16x32_bf16 v[92:95], v[196:199], v[140:143], v[92:95]\n\t"
        "v_cvt_pk_bf16_f32 v133, v54, v55\n\t"
        "ds_write_b64 v237, v[132:133] offset:4032\n\t"
        "v_mfma_f32_16x16x32_bf16 v[96:99], v[200:203], v[124:127], v[96:99]\n\t"
        "v_mfma_f32_16x16x32_bf16 v[96:99], v[204:207], v[140:143], v[96:99]\n\t"
        "v_mfma_f32_16x16x32_bf16 v[68:71], v[200:203], v[120:123], v[68:71]\n\t"
        "v_mfma_f32_16x16x32_bf16 v[68:71], v[204:207], v[136:139], v[68:71]\n\t"
        "s_waitcnt lgkmcnt(0)\n\t"
        "s_barrier\n\t"
        "v_mfma_f32_16x16x32_bf16 v[72:75], v[208:211], v[120:123], v[72:75]\n\t"
        "ds_read_b128 v[176:179], v248 offset:32768\n\t"
        "v_mfma_f32_16x16x32_bf16 v[72:75], v[212:215], v[136:139], v[72:75]\n\t"
        "ds_read_b128 v[180:183], v249 offset:32768\n\t"
        "v_mfma_f32_16x16x32_bf16 v[100:103], v[208:211], v[124:127], v[100:103]\n\t"
        "ds_read_b128 v[184:187], v248 offset:34816\n\t"
        "v_mfma_f32_16x16x32_bf16 v[100:103], v[212:215], v[140:143], v[100:103]\n\t"
        "ds_read_b128 v[188:191], v249 offset:34816\n\t"
        "v_mfma_f32_16x16x32_bf16 v[104:107], v[216:219], v[124:127], v[104:107]\n\t"
        "ds_read_b128 v[192:195], v248 offset:36864\n\t"
        "v_mfma_f32_16x16x32_bf16 v[104:107], v[220:223], v[140:143], v[104:107]\n\t"
        "ds_read_b128 v[196:199], v249 offset:36864\n\t"
        "v_mfma_f32_16x16x32_bf16 v[76:79], v[216:219], v[120:123], v[76:79]\n\t"
        "ds_read_b128 v[200:203], v248 offset:38912\n\t"
        "v_mfma_f32_16x16x32_bf16 v[76:79], v[220:223], v[136:139], v[76:79]\n\t"
        "ds_read_b128 v[204:207], v249 offset:38912\n\t"
        "v_mfma_f32_16x16x32_bf16 v[80:83], v[224:227], v[120:123], v[80:83]\n\t"
        "v_mfma_f32_16x16x32_bf16 v[80:83], v[228:231], v[136:139], v[80:83]\n\t"
        "v_mfma_f32_16x16x32_bf16 v[108:111], v[224:227], v[124:127], v[108:111]\n\t"
        "v_mfma_f32_16x16x32_bf16 v[108:111], v[228:231], v[140:143], v[108:111]\n\t"
        "s_waitcnt lgkmcnt(0)\n\t"
        "v_mfma_f32_16x16x32_bf16 v[56:59], v[176:179], v[152:155], v[56:59]\n\t"
        "ds_read_b128 v[208:211], v248 offset:40960\n\t"
        "v_mfma_f32_16x16x32_bf16 v[56:59], v[180:183], v[168:171], v[56:59]\n\t"
        "ds_read_b128 v[212:215], v249 offset:40960\n\t"
        "v_mfma_f32_16x16x32_bf16 v[84:87], v[176:179], v[156:159], v[84:87]\n\t"
        "ds_read_b128 v[216:219], v248 offset:43008\n\t"
        "v_mfma_f32_16x16x32_bf16 v[84:87], v[180:183], v[172:175], v[84:87]\n\t"
        "ds_read_b128 v[220:223], v249 offset:43008\n\t"
        "v_mfma_f32_16x16x32_bf16 v[88:91], v[184:187], v[156:159], v[88:91]\n\t"
        "ds_read_b128 v[224:227], v248 offset:45056\n\t"
        "v_mfma_f32_16x16x32_bf16 v[88:91], v[188:191], v[172:175], v[88:91]\n\t"
        "ds_read_b128 v[228:231], v249 offset:45056\n\t"
        "v_mfma_f32_16x16x32_bf16 v[60:63], v[184:187], v[152:155], v[60:63]\n\t"
        "v_mfma_f32_16x16x32_bf16 v[60:63], v[188:191], v[168:171], v[60:63]\n\t"
        "v_mfma_f32_16x16x32_bf16 v[64:67], v[192:195], v[152:155], v[64:67]\n\t"
        "v_mfma_f32_16x16x32_bf16 v[64:67], v[196:199], v[168:171], v[64:67]\n\t"
        "v_mfma_f32_16x16x32_bf16 v[92:95], v[192:195], v[156:159], v[92:95]\n\t"
        "v_mfma_f32_16x16x32_bf16 v[92:95], v[196:199], v[172:175], v[92:95]\n\t"
        "v_mfma_f32_16x16x32_bf16 v[96:99], v[200:203], v[156:159], v[96:99]\n\t"
        "v_mfma_f32_16x16x32_bf16 v[96:99], v[204:207], v[172:175], v[96:99]\n\t"
        "v_mfma_f32_16x16x32_bf16 v[68:71], v[200:203], v[152:155], v[68:71]\n\t"
        "v_mfma_f32_16x16x32_bf16 v[68:71], v[204:207], v[168:171], v[68:71]\n\t"
        "s_waitcnt lgkmcnt(0)\n\t"
        "v_mfma_f32_16x16x32_bf16 v[72:75], v[208:211], v[152:155], v[72:75]\n\t"
        "v_mfma_f32_16x16x32_bf16 v[72:75], v[212:215], v[168:171], v[72:75]\n\t"
        "v_mfma_f32_16x16x32_bf16 v[100:103], v[208:211], v[156:159], v[100:103]\n\t"
        "v_cvt_pk_bf16_f32 v112, v56, v57\n\t"
        "v_cvt_pk_bf16_f32 v113, v58, v59\n\t"
        "v_mfma_f32_16x16x32_bf16 v[100:103], v[212:215], v[172:175], v[100:103]\n\t"
        "v_cvt_pk_bf16_f32 v114, v60, v61\n\t"
        "v_cvt_pk_bf16_f32 v115, v62, v63\n\t"
        "v_mfma_f32_16x16x32_bf16 v[104:107], v[216:219], v[156:159], v[104:107]\n\t"
        "ds_write_b128 v238, v[112:115] offset:0\n\t"
        "v_cvt_pk_bf16_f32 v116, v64, v65\n\t"
        "v_mfma_f32_16x16x32_bf16 v[104:107], v[220:223], v[172:175], v[104:107]\n\t"
        "v_cvt_pk_bf16_f32 v117, v66, v67\n\t"
        "v_cvt_pk_bf16_f32 v118, v68, v69\n\t"
        "v_mfma_f32_16x16x32_bf16 v[76:79], v[216:219], v[152:155], v[76:79]\n\t"
        "v_cvt_pk_bf16_f32 v119, v70, v71\n\t"
        "ds_write_b128 v238, v[116:119] offset:64\n\t"
        "v_mfma_f32_16x16x32_bf16 v[76:79], v[220:223], v[168:171], v[76:79]\n\t"
        "v_cvt_pk_bf16_f32 v112, v84, v85\n\t"
        "v_cvt_pk_bf16_f32 v113, v86, v87\n\t"
        "v_mfma_f32_16x16x32_bf16 v[80:83], v[224:227], v[152:155], v[80:83]\n\t"
        "v_cvt_pk_bf16_f32 v114, v88, v89\n\t"
        "v_cvt_pk_bf16_f32 v115, v90, v91\n\t"
        "v_mfma_f32_16x16x32_bf16 v[80:83], v[228:231], v[168:171], v[80:83]\n\t"
        "ds_write_b128 v238, v[112:115] offset:3840\n\t"
        "v_cvt_pk_bf16_f32 v116, v92, v93\n\t"
        "v_mfma_f32_16x16x32_bf16 v[108:111], v[224:227], v[156:159], v[108:111]\n\t"
        "v_cvt_pk_bf16_f32 v117, v94, v95\n\t"
        "v_cvt_pk_bf16_f32 v118, v96, v97\n\t"
        "v_mfma_f32_16x16x32_bf16 v[108:111], v[228:231], v[172:175], v[108:111]\n\t"
        "v_cvt_pk_bf16_f32 v119, v98, v99\n\t"
        "ds_write_b128 v238, v[116:119] offset:3904\n\t"
        "v_mov_b32 v206, v244\n\t"
        "v_mov_b32 v207, v245\n\t"
        "v_mov_b32 v208, v246\n\t"
        "v_mov_b32 v209, v247\n\t"
        "s_nop 3\n\t"
        "v_cvt_pk_bf16_f32 v128, v72, v73\n\t"
        "v_cvt_pk_bf16_f32 v129, v74, v75\n\t"
        "v_cvt_pk_bf16_f32 v130, v76, v77\n\t"
        "v_cvt_pk_bf16_f32 v131, v78, v79\n\t"
        "ds_write_b128 v238, v[128:131] offset:128\n\t"
        "v_cvt_pk_bf16_f32 v132, v80, v81\n\t"
        "v_cvt_pk_bf16_f32 v133, v82, v83\n\t"
        "ds_write_b64 v239, v[132:133] offset:192\n\t"
        "v_cvt_pk_bf16_f32 v128, v100, v101\n\t"
        "v_cvt_pk_bf16_f32 v129, v102, v103\n\t"
        "v_cvt_pk_bf16_f32 v130, v104, v105\n\t"
        "v_cvt_pk_bf16_f32 v131, v106, v107\n\t"
        "ds_write_b128 v238, v[128:131] offset:3968\n\t"
        "v_cvt_pk_bf16_f32 v132, v108, v109\n\t"
        "v_cvt_pk_bf16_f32 v133, v110, v111\n\t"
        "ds_write_b64 v239, v[132:133] offset:4032\n\t"
        "s_waitcnt lgkmcnt(0)\n\t"
        "L_epi_%=:\n\t"
        "s_barrier\n\t"
        "ds_read_b128 v[0:3], v208\n\t"
        "ds_read_b128 v[4:7], v206 offset:0\n\t"
        "ds_read_b128 v[8:11], v206 offset:960\n\t"
        "ds_read_b128 v[12:15], v206 offset:1920\n\t"
        "ds_read_b128 v[16:19], v206 offset:2880\n\t"
        "s_waitcnt lgkmcnt(4)\n\t"
        "s_mov_b32 exec_hi, 0\n\t"
        "buffer_store_dwordx4 v[0:3], v209, s[28:31], 0 offen \n\t"
        "s_mov_b32 exec_hi, 0x0000ffff\n\t"
        "s_mov_b32 s35, 0\n\t"
        "s_waitcnt lgkmcnt(3)\n\t"
        "buffer_store_dwordx4 v[4:7], v207, s[28:31], s35 offen nt\n\t"
        "s_mov_b32 s35, 28672\n\t"
        "s_waitcnt lgkmcnt(2)\n\t"
        "buffer_store_dwordx4 v[8:11], v207, s[28:31], s35 offen nt\n\t"
        "s_mov_b32 s35, 57344\n\t"
        "s_waitcnt lgkmcnt(1)\n\t"
        "buffer_store_dwordx4 v[12:15], v207, s[28:31], s35 offen nt\n\t"
        "s_mov_b32 s35, 86016\n\t"
        "s_waitcnt lgkmcnt(0)\n\t"
        "buffer_store_dwordx4 v[16:19], v207, s[28:31], s35 offen nt\n\t"
        "s_mov_b32 exec_hi, -1\n\t"
        "s_branch L_end_%=\n\t"
        "L_prod_%=:\n\t"
        "s_mul_i32 s38, s52, 3584\n\t"
        "v_lshlrev_b32 v204, 4, v213\n\t"
        "v_add_u32 v204, s38, v204\n\t"
        "s_add_u32 s38, s38, 32768\n\t"
        "v_lshrrev_b32 v216, 2, v213\n\t"
        "v_and_b32 v217, 3, v213\n\t"
        "v_lshlrev_b32 v217, 3, v217\n\t"
        "v_lshl_add_u32 v205, v216, 7, v217\n\t"
        "v_lshrrev_b32 v216, 3, v215\n\t"
        "v_lshlrev_b32 v216, 5, v216\n\t"
        "v_and_b32 v217, 1, v215\n\t"
        "v_lshl_add_u32 v216, v217, 4, v216\n\t"
        "v_bfe_u32 v217, v215, 1, 2\n\t"
        "v_lshl_add_u32 v216, v217, 2, v216\n\t"
        "v_lshlrev_b32 v217, 2, v215\n\t"
        "v_cmp_gt_u32 vcc, 24, v215\n\t"
        "s_nop 1\n\t"
        "v_cndmask_b32 v216, v217, v216, vcc\n\t"
        "v_bfe_u32 v217, v216, 1, 3\n\t"
        "v_xor_b32 v217, v217, v214\n\t"
        "v_lshlrev_b32 v217, 4, v217\n\t"
        "v_lshl_add_u32 v195, v216, 7, v217\n\t"
        "v_xor_b32 v196, 16, v195\n\t"
        "v_add_u32 v196, 256, v196\n\t"
        "v_lshlrev_b32 v216, 4, v213\n\t"
        "v_add_u32 v216, 140416, v216\n\t"
        "v_add_u32 v242, 32768, v195\n\t"
        "v_add_u32 v243, 32768, v196\n\t"
        "v_cndmask_b32 v242, v216, v242, s[40:41]\n\t"
        "v_cndmask_b32 v243, v216, v243, s[40:41]\n\t"
        "v_add_u32 v244, 79872, v195\n\t"
        "v_add_u32 v245, 79872, v196\n\t"
        "v_cndmask_b32 v244, v216, v244, s[40:41]\n\t"
        "v_cndmask_b32 v245, v216, v245, s[40:41]\n\t"
        "v_add_u32 v246, 126976, v195\n\t"
        "v_add_u32 v247, 126976, v196\n\t"
        "v_cndmask_b32 v246, v216, v246, s[40:41]\n\t"
        "v_cndmask_b32 v247, v216, v247, s[40:41]\n\t"
        "s_xor_b32 s4, s36, 1\n\t"
        "s_add_u32 s5, s36, 8\n\t"
        "s_xor_b32 s6, s36, 1\n\t"
        "s_xor_b32 s7, s36, 2\n\t"
        "s_lshl_b32 s8, s4, 7\n\t"
        "s_lshl_b32 s9, s5, 7\n\t"
        "s_lshl_b32 s10, s6, 7\n\t"
        "s_lshl_b32 s11, s7, 7\n\t"
        "s_lshl_b32 s38, s52, 12\n\t"
        "s_sub_u32 s35, s52, 2\n\t"
        "s_lshl_b32 s35, s35, 10\n\t"
        "s_cmp_ge_u32 s52, 3\n\t"
        "s_cselect_b32 s35, s35, 0\n\t"
        "s_sub_u32 s38, s38, s35\n\t"
        "v_lshlrev_b32 v204, 4, v213\n\t"
        "v_add_u32 v204, s38, v204\n\t"
        "s_add_u32 s38, s38, 32768\n\t"
        "s_waitcnt lgkmcnt(0)\n\t"
        "v_and_b32 v130, 31, v213\n\t"
        "s_and_b32 s35, s37, 7\n\t"
        "s_lshl_b32 s35, s35, 5\n\t"
        "s_lshl_b32 s45, s36, 8\n\t"
        "s_add_u32 s35, s35, s45\n\t"
        "v_add_u32 v130, s35, v130\n\t"
        "v_lshlrev_b32 v130, 11, v130\n\t"
        "s_lshl_b32 s35, s54, 2\n\t"
        "s_add_u32 s35, s35, s52\n\t"
        "s_mul_i32 s35, s35, 128\n\t"
        "s_min_u32 s35, s35, L_end_%=-L_pc_%=-128\n\t"
        "s_add_u32 s18, s18, s35\n\t"
        "s_addc_u32 s19, s19, 0\n\t"
        "s_load_dword s96, s[18:19], 0\n\t"
        "s_load_dword s97, s[18:19], 64\n\t"
        "s_mul_i32 s35, s37, 4096\n\t"
        "s_add_u32 s35, s35, 8388608\n\t"
        "s_add_u32 s76, s14, s35\n\t"
        "s_addc_u32 s77, s15, 0\n\t"
        "buffer_load_dwordx2 v[198:199], v210, s[76:79], 0 offen offset:2048 sc1\n\t"
        "s_mul_i32 s35, s37, 229376\n\t"
        "s_add_u32 s24, s14, s35\n\t"
        "s_addc_u32 s25, s15, 0\n\t"
        "s_cmp_lg_u32 s52, 0\n\t"
        "s_cbranch_scc1 L_nfw_%=\n\t"
        "v_lshlrev_b32 v138, 10, v213\n\t"
        "s_lshl_b32 s35, s36, 7\n\t"
        "s_mov_b64 exec, 3\n\t"
        "buffer_load_dword v139, v138, s[76:79], s35 offen sc1\n\t"
        "s_mov_b64 exec, -1\n\t"
        "L_nfw_%=:\n\t"
        "s_barrier\n\t"
        "v_bfe_u32 v136, v213, 4, 1\n\t"
        "s_lshl_b32 s35, s37, 3\n\t"
        "s_lshl_b32 s42, s52, 1\n\t"
        "s_add_u32 s35, s35, s42\n\t"
        "s_lshl_b32 s42, s36, 8\n\t"
        "s_add_u32 s35, s35, s42\n\t"
        "v_add_u32 v136, s35, v136\n\t"
        "v_lshlrev_b32 v136, 11, v136\n\t"
        "v_and_b32 v137, 15, v213\n\t"
        "v_lshl_add_u32 v136, v137, 7, v136\n\t"
        "s_mov_b32 exec_hi, 0\n\t"
        "buffer_load_dword v137, v136, s[20:23], 0 offen \n\t"
        "s_mov_b32 exec_hi, -1\n\t"
        "s_add_u32 m0, s39, 47104\n\t"
        "s_nop 0\n\t"
        "buffer_load_dwordx4 v206, s[20:23], s9 offen offset:0  lds\n\t"
        "buffer_load_dwordx4 v207, s[20:23], s9 offen offset:1024  lds\n\t"
        "buffer_load_dwordx4 v208, s[20:23], s9 offen offset:2048  lds\n\t"
        "buffer_load_dwordx4 v209, s[20:23], s9 offen offset:3072  lds\n\t"
        "s_add_u32 m0, s39, 51200\n\t"
        "s_add_u32 s46, s9, 65536\n\t"
        "buffer_load_dwordx4 v206, s[20:23], s46 offen offset:0  lds\n\t"
        "buffer_load_dwordx4 v207, s[20:23], s46 offen offset:1024  lds\n\t"
        "buffer_load_dwordx4 v208, s[20:23], s46 offen offset:2048  lds\n\t"
        "buffer_load_dwordx4 v209, s[20:23], s46 offen offset:3072  lds\n\t"
        "s_barrier\n\t"
        "s_mul_i32 s80, s5, 917504\n\t"
        "s_mul_i32 s82, s5, 14336\n\t"
        "s_lshl_b32 s83, s5, 7\n\t"
        "buffer_load_dwordx2 v[72:73], v193, s[64:67], s82 offen\n\t"
        "buffer_load_dwordx2 v[74:75], v193, s[68:71], s82 offen\n\t"
        "buffer_load_dwordx4 v[76:79], v194, s[72:75], s83 offen\n\t"
        "buffer_load_dwordx4 v[40:43], v192, s[60:63], s80 offen nt\n\t"
        "s_add_u32 s80, s80, 14336\n\t"
        "buffer_load_dwordx4 v[44:47], v192, s[60:63], s80 offen nt\n\t"
        "s_add_u32 s80, s80, 14336\n\t"
        "buffer_load_dwordx4 v[48:51], v192, s[60:63], s80 offen nt\n\t"
        "s_add_u32 s80, s80, 14336\n\t"
        "buffer_load_dwordx4 v[52:55], v192, s[60:63], s80 offen nt\n\t"
        "s_add_u32 s80, s80, 14336\n\t"
        "buffer_load_dwordx4 v[56:59], v192, s[60:63], s80 offen nt\n\t"
        "s_add_u32 s80, s80, 14336\n\t"
        "buffer_load_dwordx4 v[60:63], v192, s[60:63], s80 offen nt\n\t"
        "s_add_u32 s80, s80, 14336\n\t"
        "buffer_load_dwordx4 v[64:67], v192, s[60:63], s80 offen nt\n\t"
        "s_add_u32 s80, s80, 14336\n\t"
        "buffer_load_dwordx4 v[68:71], v192, s[60:63], s80 offen nt\n\t"
        "s_mov_b32 s87, s6\n\t"
        "s_lshl_b32 s88, s87, 7\n\t"
        "s_mul_i32 s89, s87, 14336\n\t"
        "s_waitcnt vmcnt(0)\n\t"
        "v_add_co_u32 v222, vcc, 1, v198\n\t"
        "s_nop 1\n\t"
        "v_addc_co_u32 v223, vcc, 0, v199, vcc\n\t"
        "s_add_u32 m0, s39, 94208\n\t"
        "s_nop 0\n\t"
        "buffer_load_dwordx4 v206, s[20:23], s88 offen offset:0  lds\n\t"
        "v_lshlrev_b32 v152, 16, v72\n\t"
        "v_and_b32 v153, 0xffff0000, v72\n\t"
        "v_lshlrev_b32 v156, 16, v74\n\t"
        "v_and_b32 v157, 0xffff0000, v74\n\t"
        "v_lshlrev_b32 v154, 16, v73\n\t"
        "v_and_b32 v155, 0xffff0000, v73\n\t"
        "v_lshlrev_b32 v158, 16, v75\n\t"
        "v_and_b32 v159, 0xffff0000, v75\n\t"
        "v_lshlrev_b32 v160, 16, v76\n\t"
        "v_and_b32 v161, 0xffff0000, v76\n\t"
        "buffer_load_dwordx4 v207, s[20:23], s88 offen offset:1024  lds\n\t"
        "v_lshlrev_b32 v162, 16, v77\n\t"
        "v_and_b32 v163, 0xffff0000, v77\n\t"
        "v_lshlrev_b32 v164, 16, v78\n\t"
        "v_and_b32 v165, 0xffff0000, v78\n\t"
        "v_lshlrev_b32 v166, 16, v79\n\t"
        "v_and_b32 v167, 0xffff0000, v79\n\t"
        "v_mul_f32_e64 v156, -v156, v152\n\t"
        "v_mul_f32_e64 v157, -v157, v153\n\t"
        "v_mul_f32_e64 v158, -v158, v154\n\t"
        "v_mul_f32_e64 v159, -v159, v155\n\t"
        "buffer_load_dwordx4 v208, s[20:23], s88 offen offset:2048  lds\n\t"
        "v_cvt_f32_i32 v168, v40\n\t"
        "v_cvt_f32_i32 v169, v44\n\t"
        "v_cvt_f32_i32 v170, v48\n\t"
        "v_cvt_f32_i32 v171, v52\n\t"
        "v_cvt_f32_i32 v172, v56\n\t"
        "v_cvt_f32_i32 v173, v60\n\t"
        "v_cvt_f32_i32 v174, v64\n\t"
        "v_cvt_f32_i32 v175, v68\n\t"
        "v_fma_f32 v168, v168, v152, v156\n\t"
        "v_fma_f32 v169, v169, v152, v156\n\t"
        "buffer_load_dwordx4 v209, s[20:23], s88 offen offset:3072  lds\n\t"
        "v_fma_f32 v170, v170, v152, v156\n\t"
        "v_fma_f32 v171, v171, v152, v156\n\t"
        "v_fma_f32 v172, v172, v152, v156\n\t"
        "v_fma_f32 v173, v173, v152, v156\n\t"
        "v_fma_f32 v174, v174, v152, v156\n\t"
        "v_fma_f32 v175, v175, v152, v156\n\t"
        "v_mul_f32 v168, v168, v160\n\t"
        "v_mul_f32 v169, v169, v161\n\t"
        "v_mul_f32 v170, v170, v162\n\t"
        "v_mul_f32 v171, v171, v163\n\t"
        "s_add_u32 m0, s39, 98304\n\t"
        "s_add_u32 s46, s88, 65536\n\t"
        "buffer_load_dwordx4 v206, s[20:23], s46 offen offset:0  lds\n\t"
        "v_mul_f32 v172, v172, v164\n\t"
        "v_mul_f32 v173, v173, v165\n\t"
        "v_mul_f32 v174, v174, v166\n\t"
        "v_mul_f32 v175, v175, v167\n\t"
        "v_cvt_pk_bf16_f32 v224, v168, v169\n\t"
        "v_cvt_pk_bf16_f32 v225, v170, v171\n\t"
        "v_cvt_pk_bf16_f32 v226, v172, v173\n\t"
        "v_cvt_pk_bf16_f32 v227, v174, v175\n\t"
        "ds_write_b128 v244, v[224:227] offset:0\n\t"
        "v_cvt_f32_i32 v168, v41\n\t"
        "v_cvt_f32_i32 v169, v45\n\t"
        "buffer_load_dwordx4 v207, s[20:23], s46 offen offset:1024  lds\n\t"
        "v_cvt_f32_i32 v170, v49\n\t"
        "v_cvt_f32_i32 v171, v53\n\t"
        "v_cvt_f32_i32 v172, v57\n\t"
        "v_cvt_f32_i32 v173, v61\n\t"
        "v_cvt_f32_i32 v174, v65\n\t"
        "v_cvt_f32_i32 v175, v69\n\t"
        "v_fma_f32 v168, v168, v153, v157\n\t"
        "v_fma_f32 v169, v169, v153, v157\n\t"
        "v_fma_f32 v170, v170, v153, v157\n\t"
        "v_fma_f32 v171, v171, v153, v157\n\t"
        "buffer_load_dwordx4 v208, s[20:23], s46 offen offset:2048  lds\n\t"
        "v_fma_f32 v172, v172, v153, v157\n\t"
        "v_fma_f32 v173, v173, v153, v157\n\t"
        "v_fma_f32 v174, v174, v153, v157\n\t"
        "v_fma_f32 v175, v175, v153, v157\n\t"
        "v_mul_f32 v168, v168, v160\n\t"
        "v_mul_f32 v169, v169, v161\n\t"
        "v_mul_f32 v170, v170, v162\n\t"
        "v_mul_f32 v171, v171, v163\n\t"
        "v_mul_f32 v172, v172, v164\n\t"
        "v_mul_f32 v173, v173, v165\n\t"
        "buffer_load_dwordx4 v209, s[20:23], s46 offen offset:3072  lds\n\t"
        "v_mul_f32 v174, v174, v166\n\t"
        "v_mul_f32 v175, v175, v167\n\t"
        "v_cvt_pk_bf16_f32 v228, v168, v169\n\t"
        "v_cvt_pk_bf16_f32 v229, v170, v171\n\t"
        "v_cvt_pk_bf16_f32 v230, v172, v173\n\t"
        "v_cvt_pk_bf16_f32 v231, v174, v175\n\t"
        "ds_write_b128 v244, v[228:231] offset:128\n\t"
        "v_cvt_f32_i32 v168, v42\n\t"
        "v_cvt_f32_i32 v169, v46\n\t"
        "v_cvt_f32_i32 v170, v50\n\t"
        "v_cvt_f32_i32 v171, v54\n\t"
        "v_cvt_f32_i32 v172, v58\n\t"
        "v_cvt_f32_i32 v173, v62\n\t"
        "v_cvt_f32_i32 v174, v66\n\t"
        "v_cvt_f32_i32 v175, v70\n\t"
        "v_fma_f32 v168, v168, v154, v158\n\t"
        "v_fma_f32 v169, v169, v154, v158\n\t"
        "s_mov_b32 exec_hi, 0\n\t"
        "buffer_load_dwordx2 v[220:221], v205, s[76:79], 0 offen offset:0 sc1\n\t"
        "s_mov_b32 exec_hi, -1\n\t"
        "v_fma_f32 v170, v170, v154, v158\n\t"
        "v_fma_f32 v171, v171, v154, v158\n\t"
        "v_fma_f32 v172, v172, v154, v158\n\t"
        "v_fma_f32 v173, v173, v154, v158\n\t"
        "v_fma_f32 v174, v174, v154, v158\n\t"
        "v_fma_f32 v175, v175, v154, v158\n\t"
        "v_mul_f32 v168, v168, v160\n\t"
        "v_mul_f32 v169, v169, v161\n\t"
        "v_mul_f32 v170, v170, v162\n\t"
        "v_mul_f32 v171, v171, v163\n\t"
        "v_mul_f32 v172, v172, v164\n\t"
        "v_mul_f32 v173, v173, v165\n\t"
        "v_mul_f32 v174, v174, v166\n\t"
        "v_mul_f32 v175, v175, v167\n\t"
        "v_cvt_pk_bf16_f32 v232, v168, v169\n\t"
        "v_cvt_pk_bf16_f32 v233, v170, v171\n\t"
        "v_cvt_pk_bf16_f32 v234, v172, v173\n\t"
        "v_cvt_pk_bf16_f32 v235, v174, v175\n\t"
        "ds_write_b128 v245, v[232:235] offset:0\n\t"
        "v_cvt_f32_i32 v168, v43\n\t"
        "v_cvt_f32_i32 v169, v47\n\t"
        "v_cvt_f32_i32 v170, v51\n\t"
        "v_cvt_f32_i32 v171, v55\n\t"
        "v_cvt_f32_i32 v172, v59\n\t"
        "v_cvt_f32_i32 v173, v63\n\t"
        "v_cvt_f32_i32 v174, v67\n\t"
        "v_cvt_f32_i32 v175, v71\n\t"
        "v_fma_f32 v168, v168, v155, v159\n\t"
        "v_fma_f32 v169, v169, v155, v159\n\t"
        "v_fma_f32 v170, v170, v155, v159\n\t"
        "v_fma_f32 v171, v171, v155, v159\n\t"
        "v_fma_f32 v172, v172, v155, v159\n\t"
        "v_fma_f32 v173, v173, v155, v159\n\t"
        "v_fma_f32 v174, v174, v155, v159\n\t"
        "v_fma_f32 v175, v175, v155, v159\n\t"
        "v_mul_f32 v168, v168, v160\n\t"
        "v_mul_f32 v169, v169, v161\n\t"
        "v_mul_f32 v170, v170, v162\n\t"
        "v_mul_f32 v171, v171, v163\n\t"
        "v_mul_f32 v172, v172, v164\n\t"
        "v_mul_f32 v173, v173, v165\n\t"
        "v_mul_f32 v174, v174, v166\n\t"
        "v_mul_f32 v175, v175, v167\n\t"
        "v_cvt_pk_bf16_f32 v236, v168, v169\n\t"
        "v_cvt_pk_bf16_f32 v237, v170, v171\n\t"
        "v_cvt_pk_bf16_f32 v238, v172, v173\n\t"
        "v_cvt_pk_bf16_f32 v239, v174, v175\n\t"
        "ds_write_b128 v245, v[236:239] offset:128\n\t"
        "s_waitcnt vmcnt(0) lgkmcnt(0)\n\t"
        "s_mov_b32 s91, 1048576\n\t"
        "s_mov_b32 exec_hi, 0\n\t"
        "v_cmp_ne_u64 vcc, v[220:221], v[222:223]\n\t"
        "s_mov_b32 exec_hi, -1\n\t"
        "s_nop 2\n\t"
        "s_cbranch_vccnz L_late_%=\n\t"
        "s_add_u32 m0, s38, 94208\n\t"
        "s_cmp_ge_u32 s52, 2\n\t"
        "s_cbranch_scc1 L_w3_1_%=\n\t"
        "buffer_load_dwordx4 v204, s[24:27], s89 offen offset:3072 sc1 lds\n\t"
        "L_w3_1_%=:\n\t"
        "buffer_load_dwordx4 v204, s[24:27], s89 offen offset:0 sc1 lds\n\t"
        "buffer_load_dwordx4 v204, s[24:27], s89 offen offset:1024 sc1 lds\n\t"
        "buffer_load_dwordx4 v204, s[24:27], s89 offen offset:2048 sc1 lds\n\t"
        "s_barrier\n\t"
        "s_branch L_join_%=\n\t"
        "L_late_%=:\n\t"
        "s_barrier\n\t"
        "s_mov_b32 exec_hi, 0\n\t"
        "s_mov_b32 s91, 1048576\n\t"
        "s_branch L_pchkA_%=\n\t"
        "L_pollA_%=:\n\t"
        "buffer_load_dwordx2 v[220:221], v205, s[76:79], 0 offen offset:0 sc1\n\t"
        "s_waitcnt vmcnt(0)\n\t"
        "L_pchkA_%=:\n\t"
        "v_cmp_ne_u64 vcc, v[220:221], v[222:223]\n\t"
        "s_sub_u32 s91, s91, 1\n\t"
        "s_nop 2\n\t"
        "s_cbranch_vccz L_pdoneA_%=\n\t"
        "s_cmp_eq_u32 s91, 0\n\t"
        "s_cbranch_scc1 L_pdoneA_%=\n\t"
        "s_sleep 1\n\t"
        "s_branch L_pollA_%=\n\t"
        "L_pdoneA_%=:\n\t"
        "s_mov_b32 exec_hi, -1\n\t"
        "s_add_u32 m0, s38, 94208\n\t"
        "s_cmp_ge_u32 s52, 2\n\t"
        "s_cbranch_scc1 L_w3_2_%=\n\t"
        "buffer_load_dwordx4 v204, s[24:27], s89 offen offset:3072 sc1 lds\n\t"
        "L_w3_2_%=:\n\t"
        "buffer_load_dwordx4 v204, s[24:27], s89 offen offset:0 sc1 lds\n\t"
        "buffer_load_dwordx4 v204, s[24:27], s89 offen offset:1024 sc1 lds\n\t"
        "buffer_load_dwordx4 v204, s[24:27], s89 offen offset:2048 sc1 lds\n\t"
        "L_join_%=:\n\t"
        "s_mov_b32 s87, s7\n\t"
        "s_lshl_b32 s92, s87, 7\n\t"
        "s_mul_i32 s93, s87, 14336\n\t"
        "s_add_u32 m0, s38, 0\n\t"
        "s_cmp_ge_u32 s52, 2\n\t"
        "s_cbranch_scc1 L_w3_3_%=\n\t"
        "buffer_load_dwordx4 v204, s[24:27], s93 offen offset:3072 sc1 lds\n\t"
        "L_w3_3_%=:\n\t"
        "buffer_load_dwordx4 v204, s[24:27], s93 offen offset:0 sc1 lds\n\t"
        "buffer_load_dwordx4 v204, s[24:27], s93 offen offset:1024 sc1 lds\n\t"
        "buffer_load_dwordx4 v204, s[24:27], s93 offen offset:2048 sc1 lds\n\t"
        "s_add_u32 m0, s39, 0\n\t"
        "s_nop 0\n\t"
        "buffer_load_dwordx4 v206, s[20:23], s92 offen offset:0  lds\n\t"
        "buffer_load_dwordx4 v207, s[20:23], s92 offen offset:1024  lds\n\t"
        "buffer_load_dwordx4 v208, s[20:23], s92 offen offset:2048  lds\n\t"
        "buffer_load_dwordx4 v209, s[20:23], s92 offen offset:3072  lds\n\t"
        "s_add_u32 m0, s39, 4096\n\t"
        "s_add_u32 s46, s92, 65536\n\t"
        "buffer_load_dwordx4 v206, s[20:23], s46 offen offset:0  lds\n\t"
        "buffer_load_dwordx4 v207, s[20:23], s46 offen offset:1024  lds\n\t"
        "buffer_load_dwordx4 v208, s[20:23], s46 offen offset:2048  lds\n\t"
        "buffer_load_dwordx4 v209, s[20:23], s46 offen offset:3072  lds\n\t"
        "s_waitcnt vmcnt(11)\n\t"
        "s_barrier\n\t"
        "s_and_b32 s86, s37, 7\n\t"
        "s_sub_u32 s35, s86, 5\n\t"
        "s_cmp_ge_u32 s86, 5\n\t"
        "s_cselect_b32 s86, s35, s86\n\t"
        "s_mov_b32 s86, 0\n\t"
        "s_add_u32 s87, s86, 3\n\t"
        "s_xor_b32 s87, s87, s36\n\t"
        "s_add_u32 s86, s86, 1\n\t"
        "s_cmp_eq_u32 s86, 5\n\t"
        "s_cselect_b32 s86, 0, s86\n\t"
        "s_lshl_b32 s88, s87, 7\n\t"
        "s_mul_i32 s89, s87, 14336\n\t"
        "s_add_u32 m0, s38, 47104\n\t"
        "s_cmp_ge_u32 s52, 2\n\t"
        "s_cbranch_scc1 L_w3_4_%=\n\t"
        "buffer_load_dwordx4 v204, s[24:27], s89 offen offset:3072 sc1 lds\n\t"
        "L_w3_4_%=:\n\t"
        "buffer_load_dwordx4 v204, s[24:27], s89 offen offset:0 sc1 lds\n\t"
        "buffer_load_dwordx4 v204, s[24:27], s89 offen offset:1024 sc1 lds\n\t"
        "buffer_load_dwordx4 v204, s[24:27], s89 offen offset:2048 sc1 lds\n\t"
        "s_add_u32 m0, s39, 47104\n\t"
        "s_nop 0\n\t"
        "buffer_load_dwordx4 v206, s[20:23], s88 offen offset:0  lds\n\t"
        "buffer_load_dwordx4 v207, s[20:23], s88 offen offset:1024  lds\n\t"
        "buffer_load_dwordx4 v208, s[20:23], s88 offen offset:2048  lds\n\t"
        "buffer_load_dwordx4 v209, s[20:23], s88 offen offset:3072  lds\n\t"
        "s_add_u32 m0, s39, 51200\n\t"
        "s_add_u32 s46, s88, 65536\n\t"
        "buffer_load_dwordx4 v206, s[20:23], s46 offen offset:0  lds\n\t"
        "buffer_load_dwordx4 v207, s[20:23], s46 offen offset:1024  lds\n\t"
        "buffer_load_dwordx4 v208, s[20:23], s46 offen offset:2048  lds\n\t"
        "buffer_load_dwordx4 v209, s[20:23], s46 offen offset:3072  lds\n\t"
        "s_waitcnt vmcnt(11)\n\t"
        "s_barrier\n\t"
        "s_add_u32 s87, s86, 3\n\t"
        "s_xor_b32 s87, s87, s36\n\t"
        "s_add_u32 s86, s86, 1\n\t"
        "s_cmp_eq_u32 s86, 5\n\t"
        "s_cselect_b32 s86, 0, s86\n\t"
        "s_lshl_b32 s92, s87, 7\n\t"
        "s_mul_i32 s93, s87, 14336\n\t"
        "s_add_u32 m0, s38, 94208\n\t"
        "s_cmp_ge_u32 s52, 2\n\t"
        "s_cbranch_scc1 L_w3_5_%=\n\t"
        "buffer_load_dwordx4 v204, s[24:27], s93 offen offset:3072 sc1 lds\n\t"
        "L_w3_5_%=:\n\t"
        "buffer_load_dwordx4 v204, s[24:27], s93 offen offset:0 sc1 lds\n\t"
        "buffer_load_dwordx4 v204, s[24:27], s93 offen offset:1024 sc1 lds\n\t"
        "buffer_load_dwordx4 v204, s[24:27], s93 offen offset:2048 sc1 lds\n\t"
        "s_add_u32 m0, s39, 94208\n\t"
        "s_nop 0\n\t"
        "buffer_load_dwordx4 v206, s[20:23], s92 offen offset:0  lds\n\t"
        "buffer_load_dwordx4 v207, s[20:23], s92 offen offset:1024  lds\n\t"
        "buffer_load_dwordx4 v208, s[20:23], s92 offen offset:2048  lds\n\t"
        "buffer_load_dwordx4 v209, s[20:23], s92 offen offset:3072  lds\n\t"
        "s_add_u32 m0, s39, 98304\n\t"
        "s_add_u32 s46, s92, 65536\n\t"
        "buffer_load_dwordx4 v206, s[20:23], s46 offen offset:0  lds\n\t"
        "buffer_load_dwordx4 v207, s[20:23], s46 offen offset:1024  lds\n\t"
        "buffer_load_dwordx4 v208, s[20:23], s46 offen offset:2048  lds\n\t"
        "buffer_load_dwordx4 v209, s[20:23], s46 offen offset:3072  lds\n\t"
        "s_waitcnt vmcnt(11)\n\t"
        "s_barrier\n\t"
        "s_add_u32 s87, s86, 3\n\t"
        "s_xor_b32 s87, s87, s36\n\t"
        "s_add_u32 s86, s86, 1\n\t"
        "s_cmp_eq_u32 s86, 5\n\t"
        "s_cselect_b32 s86, 0, s86\n\t"
        "s_lshl_b32 s88, s87, 7\n\t"
        "s_mul_i32 s89, s87, 14336\n\t"
        "s_add_u32 m0, s38, 0\n\t"
        "s_cmp_ge_u32 s52, 2\n\t"
        "s_cbranch_scc1 L_w3_6_%=\n\t"
        "buffer_load_dwordx4 v204, s[24:27], s89 offen offset:3072 sc1 lds\n\t"
        "L_w3_6_%=:\n\t"
        "buffer_load_dwordx4 v204, s[24:27], s89 offen offset:0 sc1 lds\n\t"
        "buffer_load_dwordx4 v204, s[24:27], s89 offen offset:1024 sc1 lds\n\t"
        "buffer_load_dwordx4 v204, s[24:27], s89 offen offset:2048 sc1 lds\n\t"
        "s_add_u32 m0, s39, 0\n\t"
        "s_nop 0\n\t"
        "buffer_load_dwordx4 v206, s[20:23], s88 offen offset:0  lds\n\t"
        "buffer_load_dwordx4 v207, s[20:23], s88 offen offset:1024  lds\n\t"
        "buffer_load_dwordx4 v208, s[20:23], s88 offen offset:2048  lds\n\t"
        "buffer_load_dwordx4 v209, s[20:23], s88 offen offset:3072  lds\n\t"
        "s_add_u32 m0, s39, 4096\n\t"
        "s_add_u32 s46, s88, 65536\n\t"
        "buffer_load_dwordx4 v206, s[20:23], s46 offen offset:0  lds\n\t"
        "buffer_load_dwordx4 v207, s[20:23], s46 offen offset:1024  lds\n\t"
        "buffer_load_dwordx4 v208, s[20:23], s46 offen offset:2048  lds\n\t"
        "buffer_load_dwordx4 v209, s[20:23], s46 offen offset:3072  lds\n\t"
        "s_waitcnt vmcnt(11)\n\t"
        "s_barrier\n\t"
        "s_add_u32 s87, s86, 3\n\t"
        "s_xor_b32 s87, s87, s36\n\t"
        "s_add_u32 s86, s86, 1\n\t"
        "s_cmp_eq_u32 s86, 5\n\t"
        "s_cselect_b32 s86, 0, s86\n\t"
        "s_lshl_b32 s92, s87, 7\n\t"
        "s_mul_i32 s93, s87, 14336\n\t"
        "s_add_u32 m0, s38, 47104\n\t"
        "s_cmp_ge_u32 s52, 2\n\t"
        "s_cbranch_scc1 L_w3_7_%=\n\t"
        "buffer_load_dwordx4 v204, s[24:27], s93 offen offset:3072 sc1 lds\n\t"
        "L_w3_7_%=:\n\t"
        "buffer_load_dwordx4 v204, s[24:27], s93 offen offset:0 sc1 lds\n\t"
        "buffer_load_dwordx4 v204, s[24:27], s93 offen offset:1024 sc1 lds\n\t"
        "buffer_load_dwordx4 v204, s[24:27], s93 offen offset:2048 sc1 lds\n\t"
        "s_add_u32 m0, s39, 47104\n\t"
        "s_nop 0\n\t"
        "buffer_load_dwordx4 v206, s[20:23], s92 offen offset:0  lds\n\t"
        "buffer_load_dwordx4 v207, s[20:23], s92 offen offset:1024  lds\n\t"
        "buffer_load_dwordx4 v208, s[20:23], s92 offen offset:2048  lds\n\t"
        "buffer_load_dwordx4 v209, s[20:23], s92 offen offset:3072  lds\n\t"
        "s_add_u32 m0, s39, 51200\n\t"
        "s_add_u32 s46, s92, 65536\n\t"
        "buffer_load_dwordx4 v206, s[20:23], s46 offen offset:0  lds\n\t"
        "buffer_load_dwordx4 v207, s[20:23], s46 offen offset:1024  lds\n\t"
        "buffer_load_dwordx4 v208, s[20:23], s46 offen offset:2048  lds\n\t"
        "buffer_load_dwordx4 v209, s[20:23], s46 offen offset:3072  lds\n\t"
        "s_waitcnt vmcnt(11)\n\t"
        "s_barrier\n\t"
        "s_add_u32 s87, s86, 3\n\t"
        "s_xor_b32 s87, s87, s36\n\t"
        "s_add_u32 s86, s86, 1\n\t"
        "s_cmp_eq_u32 s86, 5\n\t"
        "s_cselect_b32 s86, 0, s86\n\t"
        "s_lshl_b32 s88, s87, 7\n\t"
        "s_mul_i32 s89, s87, 14336\n\t"
        "s_mov_b32 exec_hi, 0\n\t"
        "buffer_load_dwordx2 v[220:221], v205, s[76:79], 0 offen offset:1024 sc1\n\t"
        "s_mov_b32 exec_hi, -1\n\t"
        "s_add_u32 m0, s38, 94208\n\t"
        "s_cmp_ge_u32 s52, 2\n\t"
        "s_cbranch_scc1 L_w3_8_%=\n\t"
        "buffer_load_dwordx4 v204, s[24:27], s89 offen offset:3072 sc1 lds\n\t"
        "L_w3_8_%=:\n\t"
        "buffer_load_dwordx4 v204, s[24:27], s89 offen offset:0 sc1 lds\n\t"
        "buffer_load_dwordx4 v204, s[24:27], s89 offen offset:1024 sc1 lds\n\t"
        "buffer_load_dwordx4 v204, s[24:27], s89 offen offset:2048 sc1 lds\n\t"
        "s_add_u32 m0, s39, 94208\n\t"
        "s_nop 0\n\t"
        "buffer_load_dwordx4 v206, s[20:23], s88 offen offset:0  lds\n\t"
        "buffer_load_dwordx4 v207, s[20:23], s88 offen offset:1024  lds\n\t"
        "buffer_load_dwordx4 v208, s[20:23], s88 offen offset:2048  lds\n\t"
        "buffer_load_dwordx4 v209, s[20:23], s88 offen offset:3072  lds\n\t"
        "s_add_u32 m0, s39, 98304\n\t"
        "s_add_u32 s46, s88, 65536\n\t"
        "buffer_load_dwordx4 v206, s[20:23], s46 offen offset:0  lds\n\t"
        "buffer_load_dwordx4 v207, s[20:23], s46 offen offset:1024  lds\n\t"
        "buffer_load_dwordx4 v208, s[20:23], s46 offen offset:2048  lds\n\t"
        "buffer_load_dwordx4 v209, s[20:23], s46 offen offset:3072  lds\n\t"
        "s_waitcnt vmcnt(12)\n\t"
        "s_barrier\n\t"
        "s_and_b32 s86, s37, 7\n\t"
        "s_sub_u32 s35, s86, 7\n\t"
        "s_cmp_ge_u32 s86, 7\n\t"
        "s_cselect_b32 s86, s35, s86\n\t"
        "s_mov_b32 s86, 0\n\t"
        "s_add_u32 s87, s86, 9\n\t"
        "s_xor_b32 s87, s87, s36\n\t"
        "s_add_u32 s86, s86, 1\n\t"
        "s_cmp_eq_u32 s86, 7\n\t"
        "s_cselect_b32 s86, 0, s86\n\t"
        "s_lshl_b32 s92, s87, 7\n\t"
        "s_mul_i32 s93, s87, 14336\n\t"
        "s_waitcnt vmcnt(11)\n\t"
        "s_mov_b32 exec_hi, 0\n\t"
        "s_mov_b32 s91, 1048576\n\t"
        "s_branch L_pchkB_%=\n\t"
        "L_pollB_%=:\n\t"
        "buffer_load_dwordx2 v[220:221], v205, s[76:79], 0 offen offset:1024 sc1\n\t"
        "s_waitcnt vmcnt(0)\n\t"
        "L_pchkB_%=:\n\t"
        "v_cmp_ne_u64 vcc, v[220:221], v[222:223]\n\t"
        "s_sub_u32 s91, s91, 1\n\t"
        "s_nop 2\n\t"
        "s_cbranch_vccz L_pdoneB_%=\n\t"
        "s_cmp_eq_u32 s91, 0\n\t"
        "s_cbranch_scc1 L_pdoneB_%=\n\t"
        "s_sleep 1\n\t"
        "s_branch L_pollB_%=\n\t"
        "L_pdoneB_%=:\n\t"
        "s_mov_b32 exec_hi, -1\n\t"
        "s_or_b32 s35, s36, s52\n\t"
        "s_cmp_lg_u32 s35, 0\n\t"
        "s_cbranch_scc1 L_noep_%=\n\t"
        "s_mov_b64 exec, 1\n\t"
        "buffer_store_dwordx2 v[222:223], v210, s[76:79], 0 offen offset:2048 sc1\n\t"
        "s_mov_b64 exec, -1\n\t"
        "L_noep_%=:\n\t"
        "s_add_u32 m0, s38, 0\n\t"
        "s_cmp_ge_u32 s52, 2\n\t"
        "s_cbranch_scc1 L_w3_9_%=\n\t"
        "buffer_load_dwordx4 v204, s[24:27], s93 offen offset:3072 sc1 lds\n\t"
        "L_w3_9_%=:\n\t"
        "buffer_load_dwordx4 v204, s[24:27], s93 offen offset:0 sc1 lds\n\t"
        "buffer_load_dwordx4 v204, s[24:27], s93 offen offset:1024 sc1 lds\n\t"
        "buffer_load_dwordx4 v204, s[24:27], s93 offen offset:2048 sc1 lds\n\t"
        "s_add_u32 m0, s39, 0\n\t"
        "s_nop 0\n\t"
        "buffer_load_dwordx4 v206, s[20:23], s92 offen offset:0  lds\n\t"
        "buffer_load_dwordx4 v207, s[20:23], s92 offen offset:1024  lds\n\t"
        "buffer_load_dwordx4 v208, s[20:23], s92 offen offset:2048  lds\n\t"
        "buffer_load_dwordx4 v209, s[20:23], s92 offen offset:3072  lds\n\t"
        "s_add_u32 m0, s39, 4096\n\t"
        "s_add_u32 s46, s92, 65536\n\t"
        "buffer_load_dwordx4 v206, s[20:23], s46 offen offset:0  lds\n\t"
        "buffer_load_dwordx4 v207, s[20:23], s46 offen offset:1024  lds\n\t"
        "buffer_load_dwordx4 v208, s[20:23], s46 offen offset:2048  lds\n\t"
        "buffer_load_dwordx4 v209, s[20:23], s46 offen offset:3072  lds\n\t"
        "s_waitcnt vmcnt(11)\n\t"
        "s_barrier\n\t"
        "s_add_u32 s87, s86, 9\n\t"
        "s_xor_b32 s87, s87, s36\n\t"
        "s_add_u32 s86, s86, 1\n\t"
        "s_cmp_eq_u32 s86, 7\n\t"
        "s_cselect_b32 s86, 0, s86\n\t"
        "s_lshl_b32 s88, s87, 7\n\t"
        "s_mul_i32 s89, s87, 14336\n\t"
        "s_add_u32 m0, s38, 47104\n\t"
        "s_cmp_ge_u32 s52, 2\n\t"
        "s_cbranch_scc1 L_w3_10_%=\n\t"
        "buffer_load_dwordx4 v204, s[24:27], s89 offen offset:3072 sc1 lds\n\t"
        "L_w3_10_%=:\n\t"
        "buffer_load_dwordx4 v204, s[24:27], s89 offen offset:0 sc1 lds\n\t"
        "buffer_load_dwordx4 v204, s[24:27], s89 offen offset:1024 sc1 lds\n\t"
        "buffer_load_dwordx4 v204, s[24:27], s89 offen offset:2048 sc1 lds\n\t"
        "s_add_u32 m0, s39, 47104\n\t"
        "s_nop 0\n\t"
        "buffer_load_dwordx4 v206, s[20:23], s88 offen offset:0  lds\n\t"
        "buffer_load_dwordx4 v207, s[20:23], s88 offen offset:1024  lds\n\t"
        "buffer_load_dwordx4 v208, s[20:23], s88 offen offset:2048  lds\n\t"
        "buffer_load_dwordx4 v209, s[20:23], s88 offen offset:3072  lds\n\t"
        "s_add_u32 m0, s39, 51200\n\t"
        "s_add_u32 s46, s88, 65536\n\t"
        "buffer_load_dwordx4 v206, s[20:23], s46 offen offset:0  lds\n\t"
        "buffer_load_dwordx4 v207, s[20:23], s46 offen offset:1024  lds\n\t"
        "buffer_load_dwordx4 v208, s[20:23], s46 offen offset:2048  lds\n\t"
        "buffer_load_dwordx4 v209, s[20:23], s46 offen offset:3072  lds\n\t"
        "s_waitcnt vmcnt(11)\n\t"
        "s_barrier\n\t"
        "s_add_u32 s87, s86, 9\n\t"
        "s_xor_b32 s87, s87, s36\n\t"
        "s_add_u32 s86, s86, 1\n\t"
        "s_cmp_eq_u32 s86, 7\n\t"
        "s_cselect_b32 s86, 0, s86\n\t"
        "s_lshl_b32 s92, s87, 7\n\t"
        "s_mul_i32 s93, s87, 14336\n\t"
        "s_add_u32 m0, s38, 94208\n\t"
        "s_cmp_ge_u32 s52, 2\n\t"
        "s_cbranch_scc1 L_w3_11_%=\n\t"
        "buffer_load_dwordx4 v204, s[24:27], s93 offen offset:3072 sc1 lds\n\t"
        "L_w3_11_%=:\n\t"
        "buffer_load_dwordx4 v204, s[24:27], s93 offen offset:0 sc1 lds\n\t"
        "buffer_load_dwordx4 v204, s[24:27], s93 offen offset:1024 sc1 lds\n\t"
        "buffer_load_dwordx4 v204, s[24:27], s93 offen offset:2048 sc1 lds\n\t"
        "s_add_u32 m0, s39, 94208\n\t"
        "s_nop 0\n\t"
        "buffer_load_dwordx4 v206, s[20:23], s92 offen offset:0  lds\n\t"
        "buffer_load_dwordx4 v207, s[20:23], s92 offen offset:1024  lds\n\t"
        "buffer_load_dwordx4 v208, s[20:23], s92 offen offset:2048  lds\n\t"
        "buffer_load_dwordx4 v209, s[20:23], s92 offen offset:3072  lds\n\t"
        "s_add_u32 m0, s39, 98304\n\t"
        "s_add_u32 s46, s92, 65536\n\t"
        "buffer_load_dwordx4 v206, s[20:23], s46 offen offset:0  lds\n\t"
        "buffer_load_dwordx4 v207, s[20:23], s46 offen offset:1024  lds\n\t"
        "buffer_load_dwordx4 v208, s[20:23], s46 offen offset:2048  lds\n\t"
        "buffer_load_dwordx4 v209, s[20:23], s46 offen offset:3072  lds\n\t"
        "s_waitcnt vmcnt(11)\n\t"
        "s_barrier\n\t"
        "s_add_u32 s87, s86, 9\n\t"
        "s_xor_b32 s87, s87, s36\n\t"
        "s_add_u32 s86, s86, 1\n\t"
        "s_cmp_eq_u32 s86, 7\n\t"
        "s_cselect_b32 s86, 0, s86\n\t"
        "s_lshl_b32 s88, s87, 7\n\t"
        "s_mul_i32 s89, s87, 14336\n\t"
        "s_add_u32 m0, s38, 0\n\t"
        "s_cmp_ge_u32 s52, 2\n\t"
        "s_cbranch_scc1 L_w3_12_%=\n\t"
        "buffer_load_dwordx4 v204, s[24:27], s89 offen offset:3072 sc1 lds\n\t"
        "L_w3_12_%=:\n\t"
        "buffer_load_dwordx4 v204, s[24:27], s89 offen offset:0 sc1 lds\n\t"
        "buffer_load_dwordx4 v204, s[24:27], s89 offen offset:1024 sc1 lds\n\t"
        "buffer_load_dwordx4 v204, s[24:27], s89 offen offset:2048 sc1 lds\n\t"
        "s_add_u32 m0, s39, 0\n\t"
        "s_nop 0\n\t"
        "buffer_load_dwordx4 v206, s[20:23], s88 offen offset:0  lds\n\t"
        "buffer_load_dwordx4 v207, s[20:23], s88 offen offset:1024  lds\n\t"
        "buffer_load_dwordx4 v208, s[20:23], s88 offen offset:2048  lds\n\t"
        "buffer_load_dwordx4 v209, s[20:23], s88 offen offset:3072  lds\n\t"
        "s_add_u32 m0, s39, 4096\n\t"
        "s_add_u32 s46, s88, 65536\n\t"
        "buffer_load_dwordx4 v206, s[20:23], s46 offen offset:0  lds\n\t"
        "buffer_load_dwordx4 v207, s[20:23], s46 offen offset:1024  lds\n\t"
        "buffer_load_dwordx4 v208, s[20:23], s46 offen offset:2048  lds\n\t"
        "buffer_load_dwordx4 v209, s[20:23], s46 offen offset:3072  lds\n\t"
        "s_waitcnt vmcnt(11)\n\t"
        "s_barrier\n\t"
        "s_add_u32 s87, s86, 9\n\t"
        "s_xor_b32 s87, s87, s36\n\t"
        "s_add_u32 s86, s86, 1\n\t"
        "s_cmp_eq_u32 s86, 7\n\t"
        "s_cselect_b32 s86, 0, s86\n\t"
        "s_lshl_b32 s92, s87, 7\n\t"
        "s_mul_i32 s93, s87, 14336\n\t"
        "s_add_u32 m0, s38, 47104\n\t"
        "s_cmp_ge_u32 s52, 2\n\t"
        "s_cbranch_scc1 L_w3_13_%=\n\t"
        "buffer_load_dwordx4 v204, s[24:27], s93 offen offset:3072 sc1 lds\n\t"
        "L_w3_13_%=:\n\t"
        "buffer_load_dwordx4 v204, s[24:27], s93 offen offset:0 sc1 lds\n\t"
        "buffer_load_dwordx4 v204, s[24:27], s93 offen offset:1024 sc1 lds\n\t"
        "buffer_load_dwordx4 v204, s[24:27], s93 offen offset:2048 sc1 lds\n\t"
        "s_add_u32 m0, s39, 47104\n\t"
        "s_nop 0\n\t"
        "buffer_load_dwordx4 v206, s[20:23], s92 offen offset:0  lds\n\t"
        "buffer_load_dwordx4 v207, s[20:23], s92 offen offset:1024  lds\n\t"
        "buffer_load_dwordx4 v208, s[20:23], s92 offen offset:2048  lds\n\t"
        "buffer_load_dwordx4 v209, s[20:23], s92 offen offset:3072  lds\n\t"
        "s_add_u32 m0, s39, 51200\n\t"
        "s_add_u32 s46, s92, 65536\n\t"
        "buffer_load_dwordx4 v206, s[20:23], s46 offen offset:0  lds\n\t"
        "buffer_load_dwordx4 v207, s[20:23], s46 offen offset:1024  lds\n\t"
        "buffer_load_dwordx4 v208, s[20:23], s46 offen offset:2048  lds\n\t"
        "buffer_load_dwordx4 v209, s[20:23], s46 offen offset:3072  lds\n\t"
        "s_waitcnt vmcnt(11)\n\t"
        "s_barrier\n\t"
        "s_add_u32 s87, s86, 9\n\t"
        "s_xor_b32 s87, s87, s36\n\t"
        "s_add_u32 s86, s86, 1\n\t"
        "s_cmp_eq_u32 s86, 7\n\t"
        "s_cselect_b32 s86, 0, s86\n\t"
        "s_lshl_b32 s88, s87, 7\n\t"
        "s_mul_i32 s89, s87, 14336\n\t"
        "s_add_u32 m0, s38, 94208\n\t"
        "s_cmp_ge_u32 s52, 2\n\t"
        "s_cbranch_scc1 L_w3_14_%=\n\t"
        "buffer_load_dwordx4 v204, s[24:27], s89 offen offset:3072 sc1 lds\n\t"
        "L_w3_14_%=:\n\t"
        "buffer_load_dwordx4 v204, s[24:27], s89 offen offset:0 sc1 lds\n\t"
        "buffer_load_dwordx4 v204, s[24:27], s89 offen offset:1024 sc1 lds\n\t"
        "buffer_load_dwordx4 v204, s[24:27], s89 offen offset:2048 sc1 lds\n\t"
        "s_add_u32 m0, s39, 94208\n\t"
        "s_nop 0\n\t"
        "buffer_load_dwordx4 v206, s[20:23], s88 offen offset:0  lds\n\t"
        "buffer_load_dwordx4 v207, s[20:23], s88 offen offset:1024  lds\n\t"
        "buffer_load_dwordx4 v208, s[20:23], s88 offen offset:2048  lds\n\t"
        "buffer_load_dwordx4 v209, s[20:23], s88 offen offset:3072  lds\n\t"
        "s_add_u32 m0, s39, 98304\n\t"
        "s_add_u32 s46, s88, 65536\n\t"
        "buffer_load_dwordx4 v206, s[20:23], s46 offen offset:0  lds\n\t"
        "buffer_load_dwordx4 v207, s[20:23], s46 offen offset:1024  lds\n\t"
        "buffer_load_dwordx4 v208, s[20:23], s46 offen offset:2048  lds\n\t"
        "buffer_load_dwordx4 v209, s[20:23], s46 offen offset:3072  lds\n\t"
        "s_waitcnt vmcnt(11)\n\t"
        "s_barrier\n\t"
        "s_add_u32 s87, s86, 9\n\t"
        "s_xor_b32 s87, s87, s36\n\t"
        "s_add_u32 s86, s86, 1\n\t"
        "s_cmp_eq_u32 s86, 7\n\t"
        "s_cselect_b32 s86, 0, s86\n\t"
        "s_lshl_b32 s92, s87, 7\n\t"
        "s_mul_i32 s93, s87, 14336\n\t"
        "s_add_u32 m0, s38, 0\n\t"
        "s_cmp_ge_u32 s52, 2\n\t"
        "s_cbranch_scc1 L_w3_15_%=\n\t"
        "buffer_load_dwordx4 v204, s[24:27], s93 offen offset:3072 sc1 lds\n\t"
        "L_w3_15_%=:\n\t"
        "buffer_load_dwordx4 v204, s[24:27], s93 offen offset:0 sc1 lds\n\t"
        "buffer_load_dwordx4 v204, s[24:27], s93 offen offset:1024 sc1 lds\n\t"
        "buffer_load_dwordx4 v204, s[24:27], s93 offen offset:2048 sc1 lds\n\t"
        "s_add_u32 m0, s39, 0\n\t"
        "s_nop 0\n\t"
        "buffer_load_dwordx4 v206, s[20:23], s92 offen offset:0  lds\n\t"
        "buffer_load_dwordx4 v207, s[20:23], s92 offen offset:1024  lds\n\t"
        "buffer_load_dwordx4 v208, s[20:23], s92 offen offset:2048  lds\n\t"
        "buffer_load_dwordx4 v209, s[20:23], s92 offen offset:3072  lds\n\t"
        "s_add_u32 m0, s39, 4096\n\t"
        "s_add_u32 s46, s92, 65536\n\t"
        "buffer_load_dwordx4 v206, s[20:23], s46 offen offset:0  lds\n\t"
        "buffer_load_dwordx4 v207, s[20:23], s46 offen offset:1024  lds\n\t"
        "buffer_load_dwordx4 v208, s[20:23], s46 offen offset:2048  lds\n\t"
        "buffer_load_dwordx4 v209, s[20:23], s46 offen offset:3072  lds\n\t"
        "s_waitcnt vmcnt(11)\n\t"
        "s_barrier\n\t"
        "s_waitcnt vmcnt(0)\n\t"
        "s_barrier\n\t"
        "s_lshl_b32 s49, s52, 6\n\t"
        "s_mul_i32 s44, s52, 7680\n\t"
        "s_add_u32 s44, s44, 47104\n\t"
        "s_movk_i32 s50, 240\n\t"
        "v_mul_u32_u24 v214, 0x1556, v213\n\t"
        "v_lshrrev_b32 v214, 16, v214\n\t"
        "v_mul_u32_u24 v215, 12, v214\n\t"
        "v_sub_u32 v215, v213, v215\n\t"
        "s_and_b32 s39, s37, 1\n\t"
        "s_lshl_b32 s41, s39, 5\n\t"
        "v_lshlrev_b32 v215, 4, v215\n\t"
        "v_add_u32 v215, s41, v215\n\t"
        "v_mad_u32_u24 v206, v214, s50, v215\n\t"
        "v_add_u32 v206, s44, v206\n\t"
        "s_lshl_b32 s35, s36, 8\n\t"
        "s_add_u32 s42, s35, s49\n\t"
        "v_add_u32 v214, s42, v214\n\t"
        "v_mul_u32_u24 v214, 7168, v214\n\t"
        "s_mul_i32 s35, s37, 224\n\t"
        "v_add3_u32 v207, v214, v215, s35\n\t"
        "s_cmp_eq_u32 s39, 0\n\t"
        "s_cselect_b32 s41, 192, 0\n\t"
        "v_lshrrev_b32 v214, 1, v213\n\t"
        "v_and_b32 v215, 1, v213\n\t"
        "v_lshlrev_b32 v215, 4, v215\n\t"
        "v_add_u32 v215, s41, v215\n\t"
        "v_mad_u32_u24 v208, v214, s50, v215\n\t"
        "v_add_u32 v208, s44, v208\n\t"
        "v_add_u32 v214, s42, v214\n\t"
        "v_mul_u32_u24 v214, 7168, v214\n\t"
        "v_add3_u32 v209, v214, v215, s35\n\t"
        "s_barrier\n\t"
        "ds_read_b128 v[0:3], v208\n\t"
        "ds_read_b128 v[4:7], v206 offset:0\n\t"
        "ds_read_b128 v[8:11], v206 offset:960\n\t"
        "ds_read_b128 v[12:15], v206 offset:1920\n\t"
        "ds_read_b128 v[16:19], v206 offset:2880\n\t"
        "ds_read_b128 v[20:23], v206 offset:3840\n\t"
        "ds_read_b128 v[24:27], v206 offset:4800\n\t"
        "ds_read_b128 v[28:31], v206 offset:5760\n\t"
        "ds_read_b128 v[32:35], v206 offset:6720\n\t"
        "s_waitcnt lgkmcnt(8)\n\t"
        "buffer_store_dwordx4 v[0:3], v209, s[28:31], 0 offen \n\t"
        "s_mov_b32 exec_hi, 0x0000ffff\n\t"
        "s_mov_b32 s35, 0\n\t"
        "s_waitcnt lgkmcnt(7)\n\t"
        "buffer_store_dwordx4 v[4:7], v207, s[28:31], s35 offen nt\n\t"
        "s_mov_b32 s35, 28672\n\t"
        "s_waitcnt lgkmcnt(6)\n\t"
        "buffer_store_dwordx4 v[8:11], v207, s[28:31], s35 offen nt\n\t"
        "s_mov_b32 s35, 57344\n\t"
        "s_waitcnt lgkmcnt(5)\n\t"
        "buffer_store_dwordx4 v[12:15], v207, s[28:31], s35 offen nt\n\t"
        "s_mov_b32 s35, 86016\n\t"
        "s_waitcnt lgkmcnt(4)\n\t"
        "buffer_store_dwordx4 v[16:19], v207, s[28:31], s35 offen nt\n\t"
        "s_mov_b32 s35, 114688\n\t"
        "s_waitcnt lgkmcnt(3)\n\t"
        "buffer_store_dwordx4 v[20:23], v207, s[28:31], s35 offen nt\n\t"
        "s_mov_b32 s35, 143360\n\t"
        "s_waitcnt lgkmcnt(2)\n\t"
        "buffer_store_dwordx4 v[24:27], v207, s[28:31], s35 offen nt\n\t"
        "s_mov_b32 s35, 172032\n\t"
        "s_waitcnt lgkmcnt(1)\n\t"
        "buffer_store_dwordx4 v[28:31], v207, s[28:31], s35 offen nt\n\t"
        "s_mov_b32 s35, 200704\n\t"
        "s_waitcnt lgkmcnt(0)\n\t"
        "buffer_store_dwordx4 v[32:35], v207, s[28:31], s35 offen nt\n\t"
        "s_mov_b32 exec_hi, -1\n\t"
        "s_lshl_b32 s49, s52, 6\n\t"
        "s_add_u32 s49, s49, 48\n\t"
        "s_mul_i32 s35, s52, 7680\n\t"
        "s_add_u32 s44, s35, 81664\n\t"
        "s_add_u32 s43, s35, 130816\n\t"
        "s_cmp_ge_u32 s52, 2\n\t"
        "s_cselect_b32 s44, s43, s44\n\t"
        "s_movk_i32 s50, 240\n\t"
        "v_mul_u32_u24 v214, 0x1556, v213\n\t"
        "v_lshrrev_b32 v214, 16, v214\n\t"
        "v_mul_u32_u24 v215, 12, v214\n\t"
        "v_sub_u32 v215, v213, v215\n\t"
        "s_and_b32 s39, s37, 1\n\t"
        "s_lshl_b32 s41, s39, 5\n\t"
        "v_lshlrev_b32 v215, 4, v215\n\t"
        "v_add_u32 v215, s41, v215\n\t"
        "v_mad_u32_u24 v206, v214, s50, v215\n\t"
        "v_add_u32 v206, s44, v206\n\t"
        "s_lshl_b32 s35, s36, 8\n\t"
        "s_add_u32 s42, s35, s49\n\t"
        "v_add_u32 v214, s42, v214\n\t"
        "v_mul_u32_u24 v214, 7168, v214\n\t"
        "s_mul_i32 s35, s37, 224\n\t"
        "v_add3_u32 v207, v214, v215, s35\n\t"
        "s_cmp_eq_u32 s39, 0\n\t"
        "s_cselect_b32 s41, 192, 0\n\t"
        "v_lshrrev_b32 v214, 1, v213\n\t"
        "v_and_b32 v215, 1, v213\n\t"
        "v_lshlrev_b32 v215, 4, v215\n\t"
        "v_add_u32 v215, s41, v215\n\t"
        "v_mad_u32_u24 v208, v214, s50, v215\n\t"
        "v_add_u32 v208, s44, v208\n\t"
        "v_add_u32 v214, s42, v214\n\t"
        "v_mul_u32_u24 v214, 7168, v214\n\t"
        "v_add3_u32 v209, v214, v215, s35\n\t"
        "s_branch L_epi_%=\n\t"
        "L_end_%=:\n\t"
        :: [ka] "s"(ka), [bid] "s"(blockIdx.x), [tid] "v"(threadIdx.x), [l0] "s"(l0)
        : "v0", "v1", "v2", "v3", "v4", "v5", "v6", "v7", "v8", "v9", "v10", "v11", "v12", "v13", "v14", "v15", "v16", "v17", "v18", "v19", "v20", "v21", "v22", "v23", "v24", "v25", "v26", "v27", "v28", "v29", "v30", "v31", "v32", "v33", "v34", "v35", "v36", "v37", "v38", "v39", "v40", "v41", "v42", "v43", "v44", "v45", "v46", "v47", "v48", "v49", "v50", "v51", "v52", "v53", "v54", "v55", "v56", "v57", "v58", "v59", "v60", "v61", "v62", "v63", "v64", "v65", "v66", "v67", "v68", "v69", "v70", "v71", "v72", "v73", "v74", "v75", "v76", "v77", "v78", "v79", "v80", "v81", "v82", "v83", "v84", "v85", "v86", "v87", "v88", "v89", "v90", "v91", "v92", "v93", "v94", "v95", "v96", "v97", "v98", "v99", "v100", "v101", "v102", "v103", "v104", "v105", "v106", "v107", "v108", "v109", "v110", "v111", "v112", "v113", "v114", "v115", "v116", "v117", "v118", "v119", "v120", "v121", "v122", "v123", "v124", "v125", "v126", "v127", "v128", "v129", "v130", "v131", "v132", "v133", "v134", "v135", "v136", "v137", "v138", "v139", "v140", "v141", "v142", "v143", "v144", "v145", "v146", "v147", "v148", "v149", "v150", "v151", "v152", "v153", "v154", "v155", "v156", "v157", "v158", "v159", "v160", "v161", "v162", "v163", "v164", "v165", "v166", "v167", "v168", "v169", "v170", "v171", "v172", "v173", "v174", "v175", "v176", "v177", "v178", "v179", "v180", "v181", "v182", "v183", "v184", "v185", "v186", "v187", "v188", "v189", "v190", "v191", "v192", "v193", "v194", "v195", "v196", "v197", "v198", "v199", "v200", "v201", "v202", "v203", "v204", "v205", "v206", "v207", "v208", "v209", "v210", "v211", "v212", "v213", "v214", "v215", "v216", "v217", "v218", "v219", "v220", "v221", "v222", "v223", "v224", "v225", "v226", "v227", "v228", "v229", "v230", "v231", "v232", "v233", "v234", "v235", "v236", "v237", "v238", "v239", "v240", "v241", "v242", "v243", "v244", "v245", "v246", "v247", "v248", "v249", "v250", "v251", "v252", "v253", "s4", "s5", "s6", "s7", "s8", "s9", "s10", "s11", "s12", "s13", "s14", "s15", "s16", "s17", "s18", "s19", "s20", "s21", "s22", "s23", "s24", "s25", "s26", "s27", "s28", "s29", "s30", "s31", "s35", "s36", "s37", "s38", "s39", "s40", "s41", "s42", "s43", "s44", "s45", "s46", "s47", "s48", "s49", "s50", "s51", "s52", "s53", "s54", "s55", "s56", "s57", "s58", "s59", "s60", "s61", "s62", "s63", "s64", "s65", "s66", "s67", "s68", "s69", "s70", "s71", "s72", "s73", "s74", "s75", "s76", "s77", "s78", "s79", "s80", "s81", "s82", "s83", "s84", "s85", "s86", "s87", "s88", "s89", "s90", "s91", "s92", "s93", "s94", "s95", "s96", "s97", "s98", "s99", "vcc", "scc", "memory");
}

extern "C" void kernel_launch(void* const* d_in, const int* in_sizes, int n_in, void* d_out, int out_size, void* d_ws, size_t ws_size, hipStream_t stream) {
    wq_gemm_kernel<<<256, 512, 0, stream>>>(d_in[0], d_in[1], d_in[2], d_in[3], d_in[4], d_ws, d_out);
}
```
